```python
import torch

def _softmax(x, axis=-1):
    return torch.softmax(x, dim=axis)

def _gelu(x):
    return torch.nn.functional.gelu(x, approximate='tanh')

def _silu(x):
    return torch.nn.functional.silu(x)

def _sigmoid(x):
    return torch.sigmoid(x)

def _layernorm(x, axis=-1, eps=1e-05):
    m = x.mean(dim=axis, keepdim=True)
    v = x.var(dim=axis, keepdim=True, unbiased=False)
    return (x - m) / torch.sqrt(v + eps)

def _scatter_add(tgt, idx, src, axis=0):
    return tgt.clone().index_add_(axis, idx, src)

def _segment_sum(src, seg, n):
    out = torch.zeros((n,) + src.shape[1:], dtype=src.dtype)
    return out.index_add_(0, seg, src)

def _pad(x, pw):
    flat = tuple((v for pair in reversed(pw) for v in pair))
    return torch.nn.functional.pad(x, flat)
kernel_name = 'adaLN_cond__gqa_block_cached__swiglu_ffn__prompt'

def setup_inputs():

    _w = lambda a: (lambda t: t.to(torch.bfloat16) if t.is_floating_point() else t)(torch.as_tensor(a))

    def _A_setup():
        B, Sq, D = (1, 2048, 1024)
        Hq, Hkv, Sk, Dh = (32, 4, 2048, 128)
        assert Hq % Hkv == 0, (Hq, Hkv)
        return {'x': _w((lambda *s: torch.randn(*s, dtype=torch.bfloat16) * 0.05)(B, Sq, D)), 'k_cache': _w((lambda *s: torch.randn(*s, dtype=torch.bfloat16) * 3.2)(B, Hkv, Sk, Dh)), 'v_cache': _w((lambda *s: torch.randn(*s, dtype=torch.bfloat16) * 0.4)(B, Hkv, Sk, Dh)), 'wq': _w((lambda *s: torch.randn(*s, dtype=torch.bfloat16) * 3.2)(D, Hq * Dh) * D ** (-0.5)), 'wo': _w((lambda *s: torch.randn(*s, dtype=torch.bfloat16) * 0.4)(Hq * Dh, D) * (Hq * Dh) ** (-0.5))}
    _A_in = _A_setup()

    def _B_setup():
        B, S, D, F = (1, 2048, 1024, 3584)
        return {'x': _w((lambda *s: torch.randn(*s, dtype=torch.bfloat16) * 0.05)(B, S, D)), 'w_gate': _w((lambda *s: torch.randn(*s, dtype=torch.bfloat16) * 0.05)(D, F)), 'w_up': _w((lambda *s: torch.randn(*s, dtype=torch.bfloat16) * 0.05)(D, F)), 'w_down': _w((lambda *s: torch.randn(*s, dtype=torch.bfloat16) * 0.05)(F, D))}
    _B_in = _B_setup()
    return {'A_x': _A_in['x'], 'A_k_cache': _A_in['k_cache'], 'A_v_cache': _A_in['v_cache'], 'A_wq': _A_in['wq'], 'A_wo': _A_in['wo'], 'B_x': _B_in['x'], 'B_w_gate': _B_in['w_gate'], 'B_w_up': _B_in['w_up'], 'B_w_down': _B_in['w_down'], 'c': _w((lambda *s: torch.randn(*s, dtype=torch.bfloat16) * 0.1)(1, 1024) * 8.0), 'w_mod': _w((lambda *s: torch.randn(*s, dtype=torch.bfloat16) * 0.1)(1024, 6 * 1024) * 1024 ** (-0.5) * 8.0)}

def reference(A_x, A_k_cache, A_v_cache, A_wq, A_wo, B_x, B_w_gate, B_w_up, B_w_down, c, w_mod):

    def _A_ref(x, k_cache, v_cache, wq, wo):
        B, Sq, D = x.shape
        Hq, Dh = (32, 128)
        Hkv = k_cache.shape[1]
        rep = Hq // Hkv
        q = (lambda x, *d: x.permute(*d))((x @ wq).reshape(B, Sq, Hq, Dh), 0, 2, 1, 3)
        k = (lambda x, n, axis: torch.repeat_interleave(x, n, dim=axis))(k_cache, rep, axis=1)
        v = (lambda x, n, axis: torch.repeat_interleave(x, n, dim=axis))(v_cache, rep, axis=1)
        scores = torch.einsum('bhqd,bhkd->bhqk', q, k) * Dh ** (-0.5)
        p = _softmax(scores, axis=-1)
        o = torch.einsum('bhqk,bhkd->bhqd', p, v)
        return (lambda x, *d: x.permute(*d))(o, 0, 2, 1, 3).reshape(B, Sq, Hq * Dh) @ wo

    def _B_ref(x, w_gate, w_up, w_down):
        g = _silu(x @ w_gate)
        u = x @ w_up
        return g * u @ w_down
    x = A_x
    mod = (_silu(c) @ w_mod).reshape(c.shape[0], 6, 1, -1)
    sa, ca, ga, sb, cb, gb = (mod[:, 0].clone(), mod[:, 1].clone(), mod[:, 2].clone(), mod[:, 3].clone(), mod[:, 4].clone(), mod[:, 5].clone())
    h = _layernorm(x, axis=-1) * (1 + ca) + sa
    x = x + ga * _A_ref(h, A_k_cache, A_v_cache, A_wq, A_wo)
    h = _layernorm(x, axis=-1) * (1 + cb) + sb
    x = x + gb * _B_ref(h, B_w_gate, B_w_up, B_w_down)
    return x
```

```cpp
#include <hip/hip_runtime.h>
#include <hip/hip_bf16.h>
#include <utility>

constexpr int S = 2048, DM = 1024, HQ = 32, HKV = 4, D = 128, FF = 3584;
constexpr int KVBLK = 64, NT = S / KVBLK;
#define ATTN_THR 60.0f
constexpr float THR = ATTN_THR;
constexpr unsigned KRS = 256u, QRS = HQ * 256u, KTILEB = 64u * KRS;
constexpr size_t SHM_ATTN = 163840;
typedef unsigned short bf16_t;
typedef float    f32x16 __attribute__((ext_vector_type(16)));
typedef float    f32x2  __attribute__((ext_vector_type(2)));
typedef float    f32x4  __attribute__((ext_vector_type(4)));
typedef __bf16   bf16x8 __attribute__((ext_vector_type(8)));
typedef __bf16   bf16x4 __attribute__((ext_vector_type(4)));
typedef __bf16   bf16x2 __attribute__((ext_vector_type(2)));
typedef unsigned u32x4  __attribute__((ext_vector_type(4)));
typedef unsigned u32x2  __attribute__((ext_vector_type(2)));
template <int... Is> using iseq = std::integer_sequence<int, Is...>;
template <int N> using make_iseq = std::make_integer_sequence<int, N>;

#define KRING 0x0000u
#define VRING 0x8000u
#define QSTAGE 0x10000u
#define SRD_WORD3 0x00020000
#define DI __device__ __forceinline__
#define GAP() __builtin_amdgcn_sched_barrier(0)
#define W4_ACLOB_HI "a128","a129","a130","a131","a132","a133","a134","a135","a136","a137","a138","a139","a140","a141","a142","a143","a144","a145","a146","a147","a148","a149","a150","a151","a152","a153","a154","a155","a156","a157","a158","a159","a160","a161","a162","a163","a164","a165","a166","a167","a168","a169","a170","a171","a172","a173","a174","a175","a176","a177","a178","a179","a180","a181","a182","a183","a184","a185","a186","a187","a188","a189","a190","a191","a192","a193","a194","a195","a196","a197","a198","a199","a200","a201","a202","a203","a204","a205","a206","a207","a208","a209","a210","a211","a212","a213","a214","a215","a216","a217","a218","a219","a220","a221","a222","a223","a224","a225","a226","a227","a228","a229","a230","a231","a232","a233","a234","a235","a236","a237","a238","a239","a240","a241","a242","a243","a244","a245","a246","a247","a248","a249","a250","a251","a252","a253","a254","a255"
#define W4_ACLOB "a0","a1","a2","a3","a4","a5","a6","a7","a8","a9","a10","a11","a12","a13","a14","a15","a16","a17","a18","a19","a20","a21","a22","a23","a24","a25","a26","a27","a28","a29","a30","a31","a32","a33","a34","a35","a36","a37","a38","a39","a40","a41","a42","a43","a44","a45","a46","a47","a48","a49","a50","a51","a52","a53","a54","a55","a56","a57","a58","a59","a60","a61","a62","a63","a64","a65","a66","a67","a68","a69","a70","a71","a72","a73","a74","a75","a76","a77","a78","a79","a80","a81","a82","a83","a84","a85","a86","a87","a88","a89","a90","a91","a92","a93","a94","a95","a96","a97","a98","a99","a100","a101","a102","a103","a104","a105","a106","a107","a108","a109","a110","a111","a112","a113","a114","a115","a116","a117","a118","a119","a120","a121","a122","a123","a124","a125","a126","a127"
#define W4_READ_O(DB, QB, o) do { switch (2 * (DB) + (QB)) { \
    case 0: asm volatile("v_accvgpr_read_b32 %0, a0\n\t" "v_accvgpr_read_b32 %1, a1\n\t" "v_accvgpr_read_b32 %2, a2\n\t" "v_accvgpr_read_b32 %3, a3\n\t" "v_accvgpr_read_b32 %4, a4\n\t" "v_accvgpr_read_b32 %5, a5\n\t" "v_accvgpr_read_b32 %6, a6\n\t" "v_accvgpr_read_b32 %7, a7\n\t" "v_accvgpr_read_b32 %8, a8\n\t" "v_accvgpr_read_b32 %9, a9\n\t" "v_accvgpr_read_b32 %10, a10\n\t" "v_accvgpr_read_b32 %11, a11\n\t" "v_accvgpr_read_b32 %12, a12\n\t" "v_accvgpr_read_b32 %13, a13\n\t" "v_accvgpr_read_b32 %14, a14\n\t" "v_accvgpr_read_b32 %15, a15\n\t" : "=v"((o)[0]), "=v"((o)[1]), "=v"((o)[2]), "=v"((o)[3]), "=v"((o)[4]), "=v"((o)[5]), "=v"((o)[6]), "=v"((o)[7]), "=v"((o)[8]), "=v"((o)[9]), "=v"((o)[10]), "=v"((o)[11]), "=v"((o)[12]), "=v"((o)[13]), "=v"((o)[14]), "=v"((o)[15])); break; \
    case 1: asm volatile("v_accvgpr_read_b32 %0, a16\n\t" "v_accvgpr_read_b32 %1, a17\n\t" "v_accvgpr_read_b32 %2, a18\n\t" "v_accvgpr_read_b32 %3, a19\n\t" "v_accvgpr_read_b32 %4, a20\n\t" "v_accvgpr_read_b32 %5, a21\n\t" "v_accvgpr_read_b32 %6, a22\n\t" "v_accvgpr_read_b32 %7, a23\n\t" "v_accvgpr_read_b32 %8, a24\n\t" "v_accvgpr_read_b32 %9, a25\n\t" "v_accvgpr_read_b32 %10, a26\n\t" "v_accvgpr_read_b32 %11, a27\n\t" "v_accvgpr_read_b32 %12, a28\n\t" "v_accvgpr_read_b32 %13, a29\n\t" "v_accvgpr_read_b32 %14, a30\n\t" "v_accvgpr_read_b32 %15, a31\n\t" : "=v"((o)[0]), "=v"((o)[1]), "=v"((o)[2]), "=v"((o)[3]), "=v"((o)[4]), "=v"((o)[5]), "=v"((o)[6]), "=v"((o)[7]), "=v"((o)[8]), "=v"((o)[9]), "=v"((o)[10]), "=v"((o)[11]), "=v"((o)[12]), "=v"((o)[13]), "=v"((o)[14]), "=v"((o)[15])); break; \
    case 2: asm volatile("v_accvgpr_read_b32 %0, a32\n\t" "v_accvgpr_read_b32 %1, a33\n\t" "v_accvgpr_read_b32 %2, a34\n\t" "v_accvgpr_read_b32 %3, a35\n\t" "v_accvgpr_read_b32 %4, a36\n\t" "v_accvgpr_read_b32 %5, a37\n\t" "v_accvgpr_read_b32 %6, a38\n\t" "v_accvgpr_read_b32 %7, a39\n\t" "v_accvgpr_read_b32 %8, a40\n\t" "v_accvgpr_read_b32 %9, a41\n\t" "v_accvgpr_read_b32 %10, a42\n\t" "v_accvgpr_read_b32 %11, a43\n\t" "v_accvgpr_read_b32 %12, a44\n\t" "v_accvgpr_read_b32 %13, a45\n\t" "v_accvgpr_read_b32 %14, a46\n\t" "v_accvgpr_read_b32 %15, a47\n\t" : "=v"((o)[0]), "=v"((o)[1]), "=v"((o)[2]), "=v"((o)[3]), "=v"((o)[4]), "=v"((o)[5]), "=v"((o)[6]), "=v"((o)[7]), "=v"((o)[8]), "=v"((o)[9]), "=v"((o)[10]), "=v"((o)[11]), "=v"((o)[12]), "=v"((o)[13]), "=v"((o)[14]), "=v"((o)[15])); break; \
    case 3: asm volatile("v_accvgpr_read_b32 %0, a48\n\t" "v_accvgpr_read_b32 %1, a49\n\t" "v_accvgpr_read_b32 %2, a50\n\t" "v_accvgpr_read_b32 %3, a51\n\t" "v_accvgpr_read_b32 %4, a52\n\t" "v_accvgpr_read_b32 %5, a53\n\t" "v_accvgpr_read_b32 %6, a54\n\t" "v_accvgpr_read_b32 %7, a55\n\t" "v_accvgpr_read_b32 %8, a56\n\t" "v_accvgpr_read_b32 %9, a57\n\t" "v_accvgpr_read_b32 %10, a58\n\t" "v_accvgpr_read_b32 %11, a59\n\t" "v_accvgpr_read_b32 %12, a60\n\t" "v_accvgpr_read_b32 %13, a61\n\t" "v_accvgpr_read_b32 %14, a62\n\t" "v_accvgpr_read_b32 %15, a63\n\t" : "=v"((o)[0]), "=v"((o)[1]), "=v"((o)[2]), "=v"((o)[3]), "=v"((o)[4]), "=v"((o)[5]), "=v"((o)[6]), "=v"((o)[7]), "=v"((o)[8]), "=v"((o)[9]), "=v"((o)[10]), "=v"((o)[11]), "=v"((o)[12]), "=v"((o)[13]), "=v"((o)[14]), "=v"((o)[15])); break; \
    case 4: asm volatile("v_accvgpr_read_b32 %0, a64\n\t" "v_accvgpr_read_b32 %1, a65\n\t" "v_accvgpr_read_b32 %2, a66\n\t" "v_accvgpr_read_b32 %3, a67\n\t" "v_accvgpr_read_b32 %4, a68\n\t" "v_accvgpr_read_b32 %5, a69\n\t" "v_accvgpr_read_b32 %6, a70\n\t" "v_accvgpr_read_b32 %7, a71\n\t" "v_accvgpr_read_b32 %8, a72\n\t" "v_accvgpr_read_b32 %9, a73\n\t" "v_accvgpr_read_b32 %10, a74\n\t" "v_accvgpr_read_b32 %11, a75\n\t" "v_accvgpr_read_b32 %12, a76\n\t" "v_accvgpr_read_b32 %13, a77\n\t" "v_accvgpr_read_b32 %14, a78\n\t" "v_accvgpr_read_b32 %15, a79\n\t" : "=v"((o)[0]), "=v"((o)[1]), "=v"((o)[2]), "=v"((o)[3]), "=v"((o)[4]), "=v"((o)[5]), "=v"((o)[6]), "=v"((o)[7]), "=v"((o)[8]), "=v"((o)[9]), "=v"((o)[10]), "=v"((o)[11]), "=v"((o)[12]), "=v"((o)[13]), "=v"((o)[14]), "=v"((o)[15])); break; \
    case 5: asm volatile("v_accvgpr_read_b32 %0, a80\n\t" "v_accvgpr_read_b32 %1, a81\n\t" "v_accvgpr_read_b32 %2, a82\n\t" "v_accvgpr_read_b32 %3, a83\n\t" "v_accvgpr_read_b32 %4, a84\n\t" "v_accvgpr_read_b32 %5, a85\n\t" "v_accvgpr_read_b32 %6, a86\n\t" "v_accvgpr_read_b32 %7, a87\n\t" "v_accvgpr_read_b32 %8, a88\n\t" "v_accvgpr_read_b32 %9, a89\n\t" "v_accvgpr_read_b32 %10, a90\n\t" "v_accvgpr_read_b32 %11, a91\n\t" "v_accvgpr_read_b32 %12, a92\n\t" "v_accvgpr_read_b32 %13, a93\n\t" "v_accvgpr_read_b32 %14, a94\n\t" "v_accvgpr_read_b32 %15, a95\n\t" : "=v"((o)[0]), "=v"((o)[1]), "=v"((o)[2]), "=v"((o)[3]), "=v"((o)[4]), "=v"((o)[5]), "=v"((o)[6]), "=v"((o)[7]), "=v"((o)[8]), "=v"((o)[9]), "=v"((o)[10]), "=v"((o)[11]), "=v"((o)[12]), "=v"((o)[13]), "=v"((o)[14]), "=v"((o)[15])); break; \
    case 6: asm volatile("v_accvgpr_read_b32 %0, a96\n\t" "v_accvgpr_read_b32 %1, a97\n\t" "v_accvgpr_read_b32 %2, a98\n\t" "v_accvgpr_read_b32 %3, a99\n\t" "v_accvgpr_read_b32 %4, a100\n\t" "v_accvgpr_read_b32 %5, a101\n\t" "v_accvgpr_read_b32 %6, a102\n\t" "v_accvgpr_read_b32 %7, a103\n\t" "v_accvgpr_read_b32 %8, a104\n\t" "v_accvgpr_read_b32 %9, a105\n\t" "v_accvgpr_read_b32 %10, a106\n\t" "v_accvgpr_read_b32 %11, a107\n\t" "v_accvgpr_read_b32 %12, a108\n\t" "v_accvgpr_read_b32 %13, a109\n\t" "v_accvgpr_read_b32 %14, a110\n\t" "v_accvgpr_read_b32 %15, a111\n\t" : "=v"((o)[0]), "=v"((o)[1]), "=v"((o)[2]), "=v"((o)[3]), "=v"((o)[4]), "=v"((o)[5]), "=v"((o)[6]), "=v"((o)[7]), "=v"((o)[8]), "=v"((o)[9]), "=v"((o)[10]), "=v"((o)[11]), "=v"((o)[12]), "=v"((o)[13]), "=v"((o)[14]), "=v"((o)[15])); break; \
    case 7: asm volatile("v_accvgpr_read_b32 %0, a112\n\t" "v_accvgpr_read_b32 %1, a113\n\t" "v_accvgpr_read_b32 %2, a114\n\t" "v_accvgpr_read_b32 %3, a115\n\t" "v_accvgpr_read_b32 %4, a116\n\t" "v_accvgpr_read_b32 %5, a117\n\t" "v_accvgpr_read_b32 %6, a118\n\t" "v_accvgpr_read_b32 %7, a119\n\t" "v_accvgpr_read_b32 %8, a120\n\t" "v_accvgpr_read_b32 %9, a121\n\t" "v_accvgpr_read_b32 %10, a122\n\t" "v_accvgpr_read_b32 %11, a123\n\t" "v_accvgpr_read_b32 %12, a124\n\t" "v_accvgpr_read_b32 %13, a125\n\t" "v_accvgpr_read_b32 %14, a126\n\t" "v_accvgpr_read_b32 %15, a127\n\t" : "=v"((o)[0]), "=v"((o)[1]), "=v"((o)[2]), "=v"((o)[3]), "=v"((o)[4]), "=v"((o)[5]), "=v"((o)[6]), "=v"((o)[7]), "=v"((o)[8]), "=v"((o)[9]), "=v"((o)[10]), "=v"((o)[11]), "=v"((o)[12]), "=v"((o)[13]), "=v"((o)[14]), "=v"((o)[15])); break; \
    } } while (0)

DI unsigned rfl(unsigned x) { return __builtin_amdgcn_readfirstlane(x); }
DI unsigned cvtpk(float lo, float hi) {
  f32x2 v = {lo, hi}; bf16x2 b = __builtin_convertvector(v, bf16x2); return __builtin_bit_cast(unsigned, b);
}
DI float max3f(float a, float b, float c) { return __builtin_elementwise_maximum(__builtin_elementwise_maximum(a, b), c); }
DI float max2f(float a, float b) { return __builtin_elementwise_maximum(a, b); }
DI float fsub_s(float a, float b) { float r; asm("v_sub_f32_e32 %0, %1, %2" : "=v"(r) : "v"(a), "v"(b)); return r; }
DI float silu_f(float g) { return g * __builtin_amdgcn_rcpf(1.0f + __builtin_amdgcn_exp2f(-1.4426950408889634f * g)); }
DI void st16_wt(void* p, const u32x4& v) { asm volatile("global_store_dwordx4 %0, %1, off sc1\n\ts_nop 1" :: "v"(p), "v"(v) : "memory"); }
constexpr unsigned QBUF_H = 0u, QBUF_W = 32768u;
#define STAMP(i)

template <unsigned HRS_, unsigned WRS_> struct QE {
  static constexpr unsigned HRS = HRS_, WQRS = WRS_;
  unsigned vh[2], vw[2][2];
  unsigned vhsrc[4], vwsrc[4];
  unsigned m0h, m0w, sofh, sofw;
  __amdgpu_buffer_rsrc_t srdH, srdW;
  bf16x4 wlo[2][8], wup[2][8]; bf16x8 hf[2][4];
};
template <int SET, int M, bool ZERO, class Q> DI void qe_mfma(Q& q, const bf16x8 (&wf)[8]) {
  constexpr int n = M >> 2, m = M & 3, ao = 4 * (8 * m + n);
  if constexpr (ZERO) asm volatile("v_mfma_f32_16x16x32_bf16 a[%c2:%c3], %0, %1, 0" :: "v"(wf[n]), "v"(q.hf[SET][m]), "i"(ao), "i"(ao + 3) : W4_ACLOB);
  else                asm volatile("v_mfma_f32_16x16x32_bf16 a[%c2:%c3], %0, %1, a[%c2:%c3]" :: "v"(wf[n]), "v"(q.hf[SET][m]), "i"(ao), "i"(ao + 3));
}
template <int KS, int I, class Q> DI void qe_read(Q& q) {
  if constexpr (I < 4) asm volatile("ds_read_b128 %0, %1 offset:%2" : "=v"(q.hf[KS][I]) : "v"(q.vh[KS]), "i"(2048 * I) : "memory");
  else { constexpr int n = (I - 4) >> 1;
    if constexpr (((I - 4) & 1) == 0) asm volatile("ds_read_b64_tr_b16 %0, %1 offset:%2" : "=v"(q.wlo[KS][n]) : "v"(q.vw[n & 1][0]), "i"(8192 * KS + 512 * (n >> 1)) : "memory");
    else                              asm volatile("ds_read_b64_tr_b16 %0, %1 offset:%2" : "=v"(q.wup[KS][n]) : "v"(q.vw[n & 1][1]), "i"(8192 * KS + 512 * (n >> 1)) : "memory"); }
}
template <int G, class Q> DI void qe_m0(Q& q) {
  asm volatile("s_mov_b32 m0, %0" :: "s"(G == 0 ? q.m0h : (G == 1 ? q.m0h + 4096u : q.m0w)) : "m0", "memory");
}
template <int P, class Q> DI void qe_dma(Q& q) {
  constexpr unsigned HRS = Q::HRS;
  if constexpr (P < 8) asm volatile("buffer_load_dwordx4 %0, %1, %2 offen offset:%3 lds" :: "v"(q.vhsrc[P & 3]), "s"(q.srdH), "s"(q.sofh + HRS * 32u * (P >> 2)), "i"(1024 * (P & 3)) : "memory");
  else                 asm volatile("buffer_load_dwordx4 %0, %1, %2 offen offset:%3 lds" :: "v"(q.vwsrc[P - 8]), "s"(q.srdW), "s"(q.sofw), "i"(1024 * (P - 8)) : "memory");
}
template <int SET, class Q> DI void qe_wait(Q& q, bf16x8 (&wf)[8]) {
  asm volatile("s_waitcnt lgkmcnt(0)" : "+v"(q.wlo[SET][0]), "+v"(q.wlo[SET][1]), "+v"(q.wlo[SET][2]), "+v"(q.wlo[SET][3]), "+v"(q.wlo[SET][4]), "+v"(q.wlo[SET][5]), "+v"(q.wlo[SET][6]), "+v"(q.wlo[SET][7]),
                                        "+v"(q.hf[SET][0]), "+v"(q.hf[SET][1]), "+v"(q.hf[SET][2]), "+v"(q.hf[SET][3]) :: "memory");
  asm volatile("" : "+v"(q.wup[SET][0]), "+v"(q.wup[SET][1]), "+v"(q.wup[SET][2]), "+v"(q.wup[SET][3]), "+v"(q.wup[SET][4]), "+v"(q.wup[SET][5]), "+v"(q.wup[SET][6]), "+v"(q.wup[SET][7]) :: "memory");
  #pragma unroll
  for (int n = 0; n < 8; ++n) wf[n] = __builtin_shufflevector(q.wlo[SET][n], q.wup[SET][n], 0, 1, 2, 3, 4, 5, 6, 7);
  asm volatile("s_nop 1" ::: "memory");
  GAP();
}
template <int KS, bool NOMM, bool ZERO, int DMA, int M, class Q> DI void qe_gap(Q& q, const bf16x8 (&wf)[8]) {
  if constexpr (!NOMM) qe_mfma<KS ^ 1, M, ZERO>(q, wf);
  if constexpr (M < 20) qe_read<KS, M>(q);
  if constexpr (DMA == 1 && M >= 19 && M < 31 && (M & 1) == 1 && ((6 * KS + (M - 19) / 2) & 3) == 0) qe_m0<(6 * KS + (M - 19) / 2) / 4>(q);
  if constexpr (DMA == 1 && M >= 20 && (M & 1) == 0) qe_dma<6 * KS + (M - 20) / 2>(q);
  GAP();
}
template <int KS, bool NOMM, bool ZERO, int DMA, class Q, int... Ms> DI void qe_kstep(Q& q, const bf16x8 (&wf)[8], iseq<Ms...>) { (qe_gap<KS, NOMM, ZERO, DMA, Ms>(q, wf), ...); }
template <bool FIRST, int DMA, class Q> DI void qe_period_body(Q& q, bf16x8 (&wf)[8]) {
  qe_kstep<0, FIRST, false, DMA>(q, wf, make_iseq<32>{}); qe_wait<0>(q, wf);
  qe_kstep<1, false, FIRST, DMA>(q, wf, make_iseq<32>{}); qe_wait<1>(q, wf);
}
template <int P, class Q> DI void qe_dma_m(Q& q) { if constexpr ((P & 3) == 0) { qe_m0<P / 4>(q); asm volatile("s_nop 0" ::: "memory"); } qe_dma<P>(q); }
template <class Q, int... Ps> DI void qe_dma_all(Q& q, iseq<Ps...>) { (qe_dma_m<Ps>(q), ...); }
template <class Q, int... Ms> DI void qe_mfma_last(Q& q, const bf16x8 (&wf)[8], iseq<Ms...>) { (qe_mfma<1, Ms, false>(q, wf), ...); }
template <int NP, int NA, unsigned HRS, unsigned WQRS, bool PERM, class HA, class HB>
DI void qe_gemm(const char* rowbase, const char* wbase, int wbytes, unsigned B0, unsigned B1, unsigned B2, HA hookA, HB hookB) {
  const unsigned lane = threadIdx.x & 63u, wid = threadIdx.x >> 6, fr = lane & 15u, fq = lane >> 4, uw = rfl(wid);
  QE<HRS, WQRS> q; bf16x8 wf[8];
  q.srdH = __builtin_amdgcn_make_buffer_rsrc((void*)rowbase, (short)0, (int)(64 * HRS), SRD_WORD3);
  q.srdW = __builtin_amdgcn_make_buffer_rsrc((void*)(wbase - 1024), (short)0, wbytes + 1024, SRD_WORD3);
  #pragma unroll
  for (int p = 0; p < 4; ++p) q.vhsrc[p] = HRS * (8u * p + (lane >> 3)) + 16u * ((lane & 7u) ^ ((4u * (p & 1) + (lane >> 4)) & 7u)) - 1024u * p;
  #pragma unroll
  for (int w = 0; w < 4; ++w) q.vwsrc[w] = 1024u + WQRS * (8u * (w >> 1) + ((lane >> 2) & 7u)) + 128u * (w & 1) + 64u * (lane >> 5) + 16u * ((lane & 3u) ^ ((2u * (w >> 1) + ((lane >> 4) & 1u)) & 3u)) - 1024u * w;
  unsigned vh0[2], vw0[2][2];
  #pragma unroll
  for (int ks = 0; ks < 2; ++ks) vh0[ks] = QBUF_H + 8192u * wid + 128u * fr + 16u * ((4u * ks + fq) ^ ((fr >> 1) & 7u));
  #pragma unroll
  for (int n = 0; n < 2; ++n)
    #pragma unroll
    for (int t = 0; t < 2; ++t) vw0[n][t] = QBUF_W + 2048u * fq + 64u * (4u * t + (fr >> 2)) +
        (PERM ? 16u * ((lane & 3u) ^ ((2u * fq + t) & 3u)) + 8u * n : 16u * ((2u * n + ((lane & 3u) >> 1)) ^ ((2u * fq + t) & 3u)) + 8u * (lane & 1u));
  auto stage_to = [&](unsigned buf, int tile) {
    q.m0h = buf + QBUF_H + 8192u * uw; q.m0w = buf + QBUF_W + 4096u * uw;
    q.sofh = 128u * (unsigned)tile; q.sofw = WQRS * (64u * (unsigned)tile + 16u * uw); };
  auto read_from = [&](unsigned buf) {
    q.vh[0] = vh0[0] + buf; q.vh[1] = vh0[1] + buf;
    #pragma unroll
    for (int n = 0; n < 2; ++n) { q.vw[n][0] = vw0[n][0] + buf; q.vw[n][1] = vw0[n][1] + buf; } };
  stage_to(B0, 0); asm volatile("s_nop 4" ::: "memory"); qe_dma_all(q, make_iseq<12>{});
  stage_to(B1, 1); qe_dma_all(q, make_iseq<12>{});
  read_from(B0); stage_to(B2, 2);
  asm volatile("s_waitcnt vmcnt(12)\n\ts_barrier" ::: "memory"); GAP();
  qe_period_body<true, 1>(q, wf);
  unsigned brd = B1, bst = B0, bnx = B2;
  for (int p = 1; p < NP - 2; ++p) {
    read_from(brd); stage_to(bst, p + 2);
    asm volatile("s_waitcnt vmcnt(12)\n\ts_barrier" ::: "memory"); GAP();
    qe_period_body<false, 1>(q, wf);
    const unsigned tmp = brd; brd = bnx; bnx = bst; bst = tmp;
  }
  read_from(brd);
  asm volatile("s_waitcnt vmcnt(12)\n\ts_barrier" ::: "memory"); GAP();
  hookA();
  qe_period_body<false, 0>(q, wf);
  read_from(bnx);
  if constexpr (NA == 4) asm volatile("s_waitcnt vmcnt(4)\n\ts_barrier" ::: "memory"); else asm volatile("s_waitcnt vmcnt(0)\n\ts_barrier" ::: "memory");
  GAP();
  hookB();
  qe_period_body<false, 0>(q, wf);
  qe_mfma_last(q, wf, make_iseq<32>{});
  asm volatile("s_nop 7\n\ts_nop 7" ::: "memory");
}
namespace a16 {
struct C16 {
  unsigned vkb[4], vvb[2], vksrc[4], vvsrc, vvsrc2, m0k, m0v, sofk, sofv;
  __amdgpu_buffer_rsrc_t srdK, srdV;
  f32x4 S[2][4][4];
  f32x4 NEGM[4], LS[4];
  bf16x8 ONE;
  bf16x8 Vf[8][2], Pk[4][2];
  bf16x4 vlo[8][2], vup[8][2];
  float mx0, mx1; bool pend;
  unsigned pm0[2], psf[2];
  const char* pfp; unsigned m0pf;
};
constexpr int AO(int db, int qb) { return 4 * (4 * db + qb); }
constexpr int AQ(int qb, int ks) { return 128 + 16 * qb + 4 * ks; }
constexpr int AK(int kb, int ks) { return 192 + 16 * kb + 4 * ks; }
template <int VS, int I> DI void tr_read(C16& c) {
  constexpr int s = I >> 4, db = (I >> 1) & 7, off = VS * 0x4000 + 8192 * s + 4096 * (I & 1) + 512 * (db >> 1);
  if constexpr ((I & 1) == 0) asm volatile("ds_read_b64_tr_b16 %0, %1 offset:%2" : "=v"(c.vlo[db][s]) : "v"(c.vvb[db & 1]), "i"(off) : "memory");
  else                        asm volatile("ds_read_b64_tr_b16 %0, %1 offset:%2" : "=v"(c.vup[db][s]) : "v"(c.vvb[db & 1]), "i"(off) : "memory");
}
template <int KS_, int I> DI void k_read(C16& c) {
  constexpr int ks = I >> 2, kb = I & 3;
  asm volatile("ds_read_b128 a[%c1:%c2], %0 offset:%c3" :: "v"(c.vkb[ks]), "i"(AK(kb, ks)), "i"(AK(kb, ks) + 3), "i"(KS_ * 0x4000 + 4096 * kb) : "memory");
}
template <int X, bool FIRST, int M> DI void qk_mfma(C16& c) {
  constexpr int ks = M >> 4, kb = (M >> 2) & 3, qb = M & 3, ak = AK(kb, ks), aq = AQ(qb, ks);
  if constexpr (ks == 0) {
    if constexpr (FIRST) asm volatile("v_mfma_f32_16x16x32_bf16 %0, a[%c1:%c2], a[%c3:%c4], 0" : "=&v"(c.S[X][kb][qb]) : "i"(ak), "i"(ak + 3), "i"(aq), "i"(aq + 3));
    else if constexpr (M == 0) asm volatile("v_mfma_f32_16x16x32_bf16 %0, a[%c2:%c3], a[%c4:%c5], %1" : "=&v"(c.S[X][kb][qb]) : "v"(c.NEGM[qb]), "i"(ak), "i"(ak + 3), "i"(aq), "i"(aq + 3) : W4_ACLOB, W4_ACLOB_HI);
    else                 asm volatile("v_mfma_f32_16x16x32_bf16 %0, a[%c2:%c3], a[%c4:%c5], %1" : "=&v"(c.S[X][kb][qb]) : "v"(c.NEGM[qb]), "i"(ak), "i"(ak + 3), "i"(aq), "i"(aq + 3));
  } else if constexpr (M == 32) asm volatile("v_mfma_f32_16x16x32_bf16 %0, a[%c1:%c2], a[%c3:%c4], %0" : "+v"(c.S[X][kb][qb]) : "i"(ak), "i"(ak + 3), "i"(aq), "i"(aq + 3) : W4_ACLOB, W4_ACLOB_HI);
  else asm volatile("v_mfma_f32_16x16x32_bf16 %0, a[%c1:%c2], a[%c3:%c4], %0" : "+v"(c.S[X][kb][qb]) : "i"(ak), "i"(ak + 3), "i"(aq), "i"(aq + 3));
}
template <int M, bool ZERO> DI void pv_mfma(C16& c) {
  constexpr int s = M >> 5, db = (M >> 2) & 7, qb = M & 3, ao = AO(db, qb);
  if constexpr (ZERO && s == 0) asm volatile("v_mfma_f32_16x16x32_bf16 a[%c2:%c3], %0, %1, 0" :: "v"(c.Vf[db][s]), "v"(c.Pk[qb][s]), "i"(ao), "i"(ao + 3));
  else if constexpr (M == 0 || M == 40) asm volatile("v_mfma_f32_16x16x32_bf16 a[%c2:%c3], %0, %1, a[%c2:%c3]" :: "v"(c.Vf[db][s]), "v"(c.Pk[qb][s]), "i"(ao), "i"(ao + 3) : W4_ACLOB, W4_ACLOB_HI);
  else                          asm volatile("v_mfma_f32_16x16x32_bf16 a[%c2:%c3], %0, %1, a[%c2:%c3]" :: "v"(c.Vf[db][s]), "v"(c.Pk[qb][s]), "i"(ao), "i"(ao + 3));
}
template <int QB, int SS> DI void rs_mfma(C16& c) { asm volatile("v_mfma_f32_16x16x32_bf16 %0, %1, %2, %0" : "+v"(c.LS[QB]) : "v"(c.ONE), "v"(c.Pk[QB][SS])); }
template <int X, int E> DI void exp1(C16& c) {
  constexpr int kb = 2 * (E >> 5) + ((E >> 2) & 1), qb = (E >> 3) & 3, r = E & 3;
  c.S[X][kb][qb][r] = __builtin_amdgcn_exp2f(c.S[X][kb][qb][r]); asm volatile("" :: "v"(c.S[X][kb][qb][r]));
}
template <int X, int Cc> DI void pack1(C16& c) {
  constexpr int s = Cc >> 4, qb = (Cc >> 2) & 3, p = Cc & 3, kb = 2 * s + (p >> 1), r = 2 * (p & 1);
  u32x4 w = __builtin_bit_cast(u32x4, c.Pk[qb][s]);
  w[p] = cvtpk(c.S[X][kb][qb][r], c.S[X][kb][qb][r + 1]);
  asm volatile("" :: "v"(w[p]));
  c.Pk[qb][s] = __builtin_bit_cast(bf16x8, w);
}
template <int X, int QB> DI float rowmax(C16& c) {
  float m = max3f(c.S[X][0][QB][0], c.S[X][0][QB][1], c.S[X][0][QB][2]);
  m = max3f(m, c.S[X][0][QB][3], c.S[X][1][QB][0]); m = max3f(m, c.S[X][1][QB][1], c.S[X][1][QB][2]); m = max3f(m, c.S[X][1][QB][3], c.S[X][2][QB][0]);
  m = max3f(m, c.S[X][2][QB][1], c.S[X][2][QB][2]); m = max3f(m, c.S[X][2][QB][3], c.S[X][3][QB][0]); m = max3f(m, c.S[X][3][QB][1], c.S[X][3][QB][2]); m = max2f(m, c.S[X][3][QB][3]);
  { auto r = __builtin_amdgcn_permlane16_swap(__float_as_uint(m), __float_as_uint(m), false, false); m = max2f(__uint_as_float(r[0]), __uint_as_float(r[1])); }
  { auto r = __builtin_amdgcn_permlane32_swap(__float_as_uint(m), __float_as_uint(m), false, false); m = max2f(__uint_as_float(r[0]), __uint_as_float(r[1])); }
  return m;
}
template <int X, int QB> DI void shift_rows(C16& c, float dl) {
  #pragma unroll
  for (int kb = 0; kb < 4; ++kb) { _Pragma("unroll") for (int r = 0; r < 4; ++r) c.S[X][kb][QB][r] = fsub_s(c.S[X][kb][QB][r], dl); }
  const float nm = c.NEGM[QB][0] - dl;
  unsigned l; asm volatile("v_mbcnt_lo_u32_b32 %0, -1, 0\n\tv_mbcnt_hi_u32_b32 %0, -1, %0" : "=v"(l));
  const float aone = l < 16u ? 1.f : 0.f;
  asm volatile("s_nop 1\n\tv_mfma_f32_16x16x4_f32 %0, %1, %2, 0" : "+v"(c.NEGM[QB]) : "v"(aone), "v"(nm));
}
template <int R> DI void o_scale1(float f) {
  float t0, t1;
  asm volatile("v_accvgpr_read_b32 %0, a[%c3]\n\tv_accvgpr_read_b32 %1, a[%c4]\n\tv_mul_f32 %0, %2, %0\n\tv_mul_f32 %1, %2, %1\n\tv_accvgpr_write_b32 a[%c3], %0\n\tv_accvgpr_write_b32 a[%c4], %1\n\t"
               "v_accvgpr_read_b32 %0, a[%c5]\n\tv_accvgpr_read_b32 %1, a[%c6]\n\tv_mul_f32 %0, %2, %0\n\tv_mul_f32 %1, %2, %1\n\tv_accvgpr_write_b32 a[%c5], %0\n\tv_accvgpr_write_b32 a[%c6], %1"
               : "=&v"(t0), "=&v"(t1) : "v"(f), "i"(R), "i"(R + 1), "i"(R + 2), "i"(R + 3));
}
template <int... Is> DI void o_scale(const f32x4 f, iseq<Is...>) { (o_scale1<4 * Is>(f[Is & 3]), ...); }

typedef __attribute__((address_space(3))) volatile f32x4 lds_f32x4;
DI lds_f32x4* lane_scratch() { extern __shared__ __attribute__((aligned(16))) char lds[]; unsigned t = threadIdx.x; asm volatile("" : "+v"(t)); return (lds_f32x4*)((unsigned)(size_t)lds + 0x20000u + 16u * t); }
constexpr int NBX = 32;
template <int X, bool FIRST, int M> DI void gapA(C16& c, const int t) {
  constexpr int Xp = X ^ 1;
  qk_mfma<X, FIRST, M>(c);
  GAP();
  if constexpr (!FIRST) {
    if constexpr (M < 48) {
      if constexpr ((M & 1) == 0) exp1<Xp, NBX + (M >> 1)>(c);
      else { constexpr int g = M >> 1;
        if constexpr (g % 3 == 0) { tr_read<Xp, 4 * (g / 3)>(c); tr_read<Xp, 4 * (g / 3) + 1>(c); }
        else if constexpr (g % 3 == 1) { tr_read<Xp, 4 * (g / 3) + 2>(c); pack1<Xp, 2 * (g / 3)>(c); }
        else { tr_read<Xp, 4 * (g / 3) + 3>(c); pack1<Xp, 2 * (g / 3) + 1>(c); } }
    } else if constexpr (M < 56) exp1<Xp, NBX + 24 + (M - 48)>(c);
    else { pack1<Xp, 16 + 2 * (M - 56)>(c); pack1<Xp, 17 + 2 * (M - 56)>(c); }
    if constexpr (M == 20) asm volatile("" :: "v"(c.NEGM[0]), "v"(c.NEGM[1]), "v"(c.NEGM[2]), "v"(c.NEGM[3]));
  }
  if constexpr (M == 49) { c.pm0[0] = c.m0k + (unsigned)(X) * 0x4000u;      asm volatile("" : "+s"(c.pm0[0])); }
  if constexpr (M == 51) { c.psf[0] = KTILEB * (unsigned)(t + 2) + c.sofk;  asm volatile("" : "+s"(c.psf[0])); }
  if constexpr (M == 53) { c.pm0[1] = c.m0v + (unsigned)(X ^ 1) * 0x4000u;  asm volatile("" : "+s"(c.pm0[1])); }
  if constexpr (M == 55) { c.psf[1] = KTILEB * (unsigned)(t + 1) + c.sofv - 896u; asm volatile("" : "+s"(c.psf[1])); }
  GAP();
}
#define A16_V(a) "+v"(c.a[0][0]), "+v"(c.a[0][1]), "+v"(c.a[1][0]), "+v"(c.a[1][1]), "+v"(c.a[2][0]), "+v"(c.a[2][1]), "+v"(c.a[3][0]), "+v"(c.a[3][1]), \
                 "+v"(c.a[4][0]), "+v"(c.a[4][1]), "+v"(c.a[5][0]), "+v"(c.a[5][1]), "+v"(c.a[6][0]), "+v"(c.a[6][1]), "+v"(c.a[7][0]), "+v"(c.a[7][1])
DI void v_concat(C16& c) {
  #pragma unroll
  for (int s = 0; s < 2; ++s)
    #pragma unroll
    for (int db = 0; db < 8; ++db) c.Vf[db][s] = __builtin_shufflevector(c.vlo[db][s], c.vup[db][s], 0, 1, 2, 3, 4, 5, 6, 7);
}
template <int X, bool FIRST, int... Ms> DI void phaseA_(C16& c, const int t, iseq<Ms...>) {
  (gapA<X, FIRST, Ms>(c, t), ...);
  if constexpr (FIRST) asm volatile("s_waitcnt vmcnt(0) lgkmcnt(0)\n\ts_barrier" ::: "memory");
  else if constexpr (X == 1) { asm volatile("s_waitcnt vmcnt(2) lgkmcnt(0)" : A16_V(vlo) :: "memory"); asm volatile("s_barrier" : A16_V(vup) :: "memory"); }
  else { asm volatile("s_waitcnt vmcnt(0) lgkmcnt(0)" : A16_V(vlo) :: "memory"); asm volatile("s_barrier" : A16_V(vup) :: "memory"); }
  GAP();
  if constexpr (!FIRST) v_concat(c);
}
template <int X, bool FIRST> DI void phaseA(C16& c, const int t) { phaseA_<X, FIRST>(c, t, make_iseq<64>{}); }

template <int X, int I> DI void max_step(C16& c) {
  constexpr int kb = I >> 3, qb = (I >> 1) & 3, r = 2 * (I & 1);
  if constexpr (I == 0) c.mx0 = max2f(c.S[X][kb][qb][r], c.S[X][kb][qb][r + 1]);
  else if constexpr (I == 1) c.mx1 = max2f(c.S[X][kb][qb][r], c.S[X][kb][qb][r + 1]);
  else if constexpr (I & 1) c.mx1 = max3f(c.mx1, c.S[X][kb][qb][r], c.S[X][kb][qb][r + 1]);
  else c.mx0 = max3f(c.mx0, c.S[X][kb][qb][r], c.S[X][kb][qb][r + 1]);
}
template <int X, bool PEEL, bool ZERO1, bool DK, bool DV, bool KR, int M> DI void gapB(C16& c) {
  if constexpr (!PEEL) {
    if constexpr (M % 9 == 8) rs_mfma<((M / 9) & 3), ((M / 9) >> 2)>(c);
    else pv_mfma<M - M / 9, ZERO1>(c);
    GAP();
  }
  if constexpr (M < 2) { if constexpr (KR) { k_read<X ^ 1, 2 * M>(c); k_read<X ^ 1, 2 * M + 1>(c); } }
  else if constexpr (M < 14) { if constexpr (KR) k_read<X ^ 1, M + 2>(c); if constexpr (!PEEL) max_step<X, M - 2>(c); }
  else if constexpr (M < 24) { if constexpr (!PEEL) { max_step<X, 12 + 2 * (M - 14)>(c); max_step<X, 13 + 2 * (M - 14)>(c); } }
  else if constexpr (M == 24) {
    if constexpr (PEEL) {
      { const float m = rowmax<X, 0>(c); shift_rows<X, 0>(c, m); } { const float m = rowmax<X, 1>(c); shift_rows<X, 1>(c, m); }
      { const float m = rowmax<X, 2>(c); shift_rows<X, 2>(c, m); } { const float m = rowmax<X, 3>(c); shift_rows<X, 3>(c, m); }
    } else {
      if (__builtin_expect(__any((int)(max2f(c.mx0, c.mx1) > THR)), 0)) {
        f32x4 f;
        { const float dl = max2f(rowmax<X, 0>(c), 0.f); shift_rows<X, 0>(c, dl); f[0] = __builtin_amdgcn_exp2f(-dl); }
        { const float dl = max2f(rowmax<X, 1>(c), 0.f); shift_rows<X, 1>(c, dl); f[1] = __builtin_amdgcn_exp2f(-dl); }
        { const float dl = max2f(rowmax<X, 2>(c), 0.f); shift_rows<X, 2>(c, dl); f[2] = __builtin_amdgcn_exp2f(-dl); }
        { const float dl = max2f(rowmax<X, 3>(c), 0.f); shift_rows<X, 3>(c, dl); f[3] = __builtin_amdgcn_exp2f(-dl); }
        *lane_scratch() = f;
        c.pend = true;
      }
    }
  } else {
    constexpr int g = M - 25;
    if constexpr (g < 40 && (g & 3) == 3) { constexpr int dsl = g >> 2;
      if constexpr (dsl == 0) { if constexpr (DK) asm volatile("s_mov_b32 m0, %0" :: "s"(c.pm0[0]) : "m0", "memory"); }
      else if constexpr (dsl < 5) { if constexpr (DK) asm volatile("buffer_load_dwordx4 %0, %1, %2 offen offset:%3 lds" :: "v"(c.vksrc[dsl - 1]), "s"(c.srdK), "s"(c.psf[0]), "i"(1024 * (dsl - 1)) : "memory"); }
      else if constexpr (dsl == 5) { if constexpr (DV) asm volatile("s_mov_b32 m0, %0" :: "s"(c.pm0[1]) : "m0", "memory"); }
      else if constexpr (DV) { constexpr int q = dsl - 6;
        if constexpr (q & 1) asm volatile("buffer_load_dwordx4 %0, %1, %2 offen offset:%3 lds" :: "v"(c.vvsrc),  "s"(c.srdV), "s"(c.psf[1]), "i"(1024 * q) : "memory");
        else                 asm volatile("buffer_load_dwordx4 %0, %1, %2 offen offset:%3 lds" :: "v"(c.vvsrc2), "s"(c.srdV), "s"(c.psf[1]), "i"(1024 * q) : "memory"); }
    } else { constexpr int e = g < 40 ? g - (g >> 2) : g - 10; if constexpr (e < NBX) exp1<X, e>(c); }
  }
  if constexpr (X == 0 && M == 67) asm volatile("s_mov_b32 m0, %0" :: "s"(c.m0pf) : "m0", "memory");
  if constexpr (X == 0 && M == 68) asm volatile("global_load_lds_dwordx4 %0, %1" :: "v"(c.vksrc[0]), "s"(c.pfp) : "memory");
  if constexpr (X == 0 && M == 70) { asm volatile("global_load_lds_dwordx4 %0, %1 offset:1024" :: "v"(c.vksrc[0]), "s"(c.pfp) : "memory"); c.pfp += 2048; }
  if constexpr (M == 71) asm volatile("" :: "v"(c.NEGM[0]), "v"(c.NEGM[1]), "v"(c.NEGM[2]), "v"(c.NEGM[3]));
  GAP();
}
template <int X, bool PEEL, bool ZERO1, bool DK, bool DV, bool KR, int... Ms> DI void phaseB_(C16& c, iseq<Ms...>) {
  if constexpr (PEEL) asm volatile("s_nop 7\n\ts_nop 7" ::: "memory"); else asm volatile("s_nop 1" ::: "memory");
  GAP();
  (gapB<X, PEEL, ZERO1, DK, DV, KR, Ms>(c), ...);
  if constexpr (!PEEL) {
    if (__builtin_expect(c.pend, 0)) {
      asm volatile("s_nop 7\n\ts_nop 7" : "+v"(c.LS[0]), "+v"(c.LS[1]), "+v"(c.LS[2]), "+v"(c.LS[3]) :: "memory");
      asm volatile("s_waitcnt lgkmcnt(0)" ::: "memory");
      const f32x4 f = *lane_scratch();
      o_scale(f, make_iseq<32>{});
      #pragma unroll
      for (int qb = 0; qb < 4; ++qb) c.LS[qb] *= f[qb];
      asm volatile("s_nop 1" : "+v"(c.LS[0]), "+v"(c.LS[1]), "+v"(c.LS[2]), "+v"(c.LS[3]) :: "memory");
      c.pend = false;
    }
  }
  asm volatile("s_waitcnt lgkmcnt(0)" ::: "memory");
  GAP();
}
template <int X, bool PEEL, bool ZERO1, bool DK = true, bool DV = true, bool KR = true> DI void phaseB(C16& c) { phaseB_<X, PEEL, ZERO1, DK, DV, KR>(c, make_iseq<72>{}); }
template <int KS_, int... Is> DI void k_reads(C16& c, iseq<Is...>) { (k_read<KS_, Is>(c), ...); }
template <int VS, int... Is> DI void tr_reads(C16& c, iseq<Is...>) { (tr_read<VS, Is>(c), ...); }
template <int X, int... Es> DI void exps_rest(C16& c, iseq<Es...>) { (exp1<X, NBX + Es>(c), ...); }
template <int X, int... Cs> DI void packs(C16& c, iseq<Cs...>) { (pack1<X, Cs>(c), ...); }
template <int... Ms> DI void pv_all(C16& c, iseq<Ms...>) { (pv_mfma<Ms, false>(c), ...); }
template <int I> DI void q_write(const u32x4& w) {
  asm volatile("v_accvgpr_write_b32 a[%c4], %0\n\tv_accvgpr_write_b32 a[%c5], %1\n\tv_accvgpr_write_b32 a[%c6], %2\n\tv_accvgpr_write_b32 a[%c7], %3"
               :: "v"(w[0]), "v"(w[1]), "v"(w[2]), "v"(w[3]), "i"(128 + 4 * I), "i"(129 + 4 * I), "i"(130 + 4 * I), "i"(131 + 4 * I) : W4_ACLOB_HI);
}
template <int I> DI void q_conv1(float C) {
  float o[16];
  W4_READ_O(I >> 1, I & 1, o);
  #pragma unroll
  for (int k2 = 0; k2 < 2; ++k2) {
    u32x4 w;
    #pragma unroll
    for (int e = 0; e < 4; ++e) w[e] = cvtpk(o[8 * k2 + 2 * e] * C, o[8 * k2 + 2 * e + 1] * C);
    if (k2 == 0) q_write<4 * (I >> 1) + 2 * (I & 1)>(w); else q_write<4 * (I >> 1) + 2 * (I & 1) + 1>(w);
  }
}
template <int... Is> DI void q_conv(float C, iseq<Is...>) { (q_conv1<Is>(C), ...); }

__global__ __launch_bounds__(256, 1)
void attn_fwd(const unsigned short* __restrict__ Hm, const unsigned short* __restrict__ Wq, const unsigned short* __restrict__ Kb, const unsigned short* __restrict__ Vb, unsigned short* __restrict__ O,
              const char* __restrict__ pf0, const char* __restrict__ pf1, const char* __restrict__ pf2, const char* __restrict__ pf3
  ) {
  extern __shared__ __attribute__((aligned(16))) char lds[];
  STAMP(0);
  const unsigned tid = threadIdx.x, lane = tid & 63u, wid = tid >> 6, fr = lane & 15u, fq = lane >> 4;
  constexpr int T = NT, GQ = HQ / HKV;
  const float C = 0.12751743082f;
  C16 c;
  const unsigned ldsb = (unsigned)(size_t)lds;
  #pragma unroll
  for (int ks = 0; ks < 4; ++ks) c.vkb[ks] = ldsb + KRING + 256u * fr + 16u * ((4u * ks + fq) ^ fr);
  #pragma unroll
  for (int o = 0; o < 2; ++o) c.vvb[o] = ldsb + VRING + 2048u * (fq >> 1) + 256u * (fq & 1u) + 64u * (fr >> 2) + 32u * (o ^ (fq & 1u)) + 8u * (lane & 3u);
  #pragma unroll
  for (int q = 0; q < 4; ++q) c.vksrc[q] = KRS * fq + 16u * (fr ^ (4u * q + fq));
  c.vvsrc = KRS * ((lane >> 2) & 7u) + 64u * (lane >> 5) + 16u * ((lane & 3u) ^ (2u * (fq & 1u)));
  c.vvsrc2 = c.vvsrc + 896u;
  { u32x4 v = {0x3F803F80u, 0x3F803F80u, 0x3F803F80u, 0x3F803F80u}; c.ONE = __builtin_bit_cast(bf16x8, v); asm volatile("" : "+v"(c.ONE)); }
  #pragma unroll
  for (int qb = 0; qb < 4; ++qb) { c.LS[qb] = (f32x4){0.f, 0.f, 0.f, 0.f}; c.NEGM[qb] = (f32x4){0.f, 0.f, 0.f, 0.f}; }
  c.pend = false;
  c.m0k = rfl(ldsb + KRING + 4096u * wid); c.m0v = rfl(ldsb + VRING + 4096u * wid);
  c.sofk = rfl(KRS * 16u * wid); c.sofv = rfl(KRS * 16u * wid);
  const unsigned ublk = blockIdx.x;
  const int  kvh = (int)(ublk & 3u), h = kvh * GQ + (int)((ublk >> 3) & 7u), y = 4 * (int)((ublk >> 2) & 1u) + (int)(ublk >> 6);
  const int  row0 = 256 * y + 64 * (int)rfl(wid);
  c.srdK = __builtin_amdgcn_make_buffer_rsrc((void*)((const char*)Kb + (size_t)kvh * S * 256), (short)0, S * 256, SRD_WORD3);
  c.srdV = __builtin_amdgcn_make_buffer_rsrc((void*)((const char*)Vb + (size_t)kvh * S * 256), (short)0, S * 256, SRD_WORD3);
  {
    const unsigned gw = ublk * 4u + rfl(wid);
    c.pfp = gw < 256u ? pf0 + (size_t)gw * 32768u : gw < 480u ? pf1 + (size_t)(gw - 256u) * 32768u : gw < 704u ? pf2 + (size_t)(gw - 480u) * 32768u : gw < 928u ? pf3 + (size_t)(gw - 704u) * 32768u : pf0;
    c.m0pf = rfl(ldsb + 0x24000u + 2048u * wid); }
  auto dma_k = [&](int tk) {
    const unsigned slot = (unsigned)(tk & 1) * 0x4000u, skv = KTILEB * (unsigned)tk;
    #pragma unroll
    for (int q = 0; q < 4; ++q)
      asm volatile("s_nop 4\n\ts_mov_b32 m0, %0\n\ts_nop 0\n\tbuffer_load_dwordx4 %1, %2, %3 offen lds"
                   :: "s"(rfl(c.m0k + slot + 1024u * q)), "v"(c.vksrc[q]), "s"(c.srdK), "s"(rfl(skv + c.sofk + KRS * 4u * q)) : "m0", "memory");
  };
  auto dma_v = [&](int tv) {
    const unsigned slot = (unsigned)(tv & 1) * 0x4000u, skv = KTILEB * (unsigned)tv;
    #pragma unroll
    for (int q = 0; q < 4; ++q)
      asm volatile("s_nop 4\n\ts_mov_b32 m0, %0\n\ts_nop 0\n\tbuffer_load_dwordx4 %1, %2, %3 offen lds"
                   :: "s"(rfl(c.m0v + slot + 1024u * q)), "v"(c.vvsrc), "s"(c.srdV), "s"(rfl(skv + c.sofv + KRS * 8u * (q >> 1) + 128u * (q & 1))) : "m0", "memory");
  };
  qe_gemm<16, 4, DM * 2u, HQ * D * 2u, true>((const char*)Hm + (size_t)row0 * (DM * 2), (const char*)Wq + (size_t)h * 256, (int)(DM * HQ * D * 2 - h * 256),
                                            ldsb + 0x10000u, ldsb + 0x1C000u, ldsb + 0x4000u, [&]() { dma_k(0); }, [&]() { dma_v(0); dma_k(1); });
  q_conv(C, make_iseq<8>{});
  asm volatile("s_waitcnt vmcnt(0)\n\ts_barrier" ::: "memory");
  k_reads<0>(c, make_iseq<16>{});
  asm volatile("s_waitcnt lgkmcnt(0)\n\ts_nop 1" : "+v"(c.LS[0]), "+v"(c.LS[1]), "+v"(c.LS[2]), "+v"(c.LS[3]) :: "memory");
  STAMP(1);
  phaseA<0, true>(c, 0);  phaseB<0, true,  false>(c);
  phaseA<1, false>(c, 1); phaseB<1, false, true>(c);
  for (int t = 2; t < T - 2; t += 2) {
    phaseA<0, false>(c, t);     phaseB<0, false, false>(c);
    phaseA<1, false>(c, t + 1); phaseB<1, false, false>(c);
  }
  phaseA<0, false>(c, T - 2); phaseB<0, false, false, false, true, true>(c);
  phaseA<1, false>(c, T - 1); phaseB<1, false, false, false, false, false>(c);
  {
    tr_reads<1>(c, make_iseq<32>{});
    exps_rest<1>(c, make_iseq<64 - NBX>{});
    packs<1>(c, make_iseq<32>{});
    asm volatile("s_waitcnt lgkmcnt(0)" : A16_V(vlo) :: "memory"); asm volatile("" : A16_V(vup) :: "memory");
    GAP();
    v_concat(c);
    asm volatile("s_nop 1" ::: "memory");
    rs_mfma<0, 0>(c); rs_mfma<1, 0>(c); rs_mfma<2, 0>(c); rs_mfma<3, 0>(c); rs_mfma<0, 1>(c); rs_mfma<1, 1>(c); rs_mfma<2, 1>(c); rs_mfma<3, 1>(c);
    pv_all(c, make_iseq<64>{});
    asm volatile("s_nop 7\n\ts_nop 7" : "+v"(c.LS[0]), "+v"(c.LS[1]), "+v"(c.LS[2]), "+v"(c.LS[3]) :: "memory");
  }
  STAMP(2);
  {
    unsigned lb;
    asm volatile("v_mbcnt_lo_u32_b32 %0, -1, 0\n\tv_mbcnt_hi_u32_b32 %0, -1, %0" : "=v"(lb));
    const unsigned frb = lb & 15u, fqb = lb >> 4;
    char* stg = lds + QSTAGE + 16384u * wid;
    float il[4];
    #pragma unroll
    for (int qb = 0; qb < 4; ++qb) il[qb] = __builtin_amdgcn_rcpf(c.LS[qb][0]);
    #pragma unroll
    for (int db = 0; db < 8; ++db) {
      float o[16];
      W4_READ_O(db >> 1, db & 1, o);
      #pragma unroll
      for (int qb = 0; qb < 4; ++qb) {
        u32x2 w = {cvtpk(o[4 * qb + 0] * il[qb], o[4 * qb + 1] * il[qb]), cvtpk(o[4 * qb + 2] * il[qb], o[4 * qb + 3] * il[qb])};
        const unsigned r = 16u * qb + frb, ch = 2u * db + (fqb >> 1);
        *(u32x2*)(stg + 256u * r + 16u * (ch ^ frb) + 8u * (fqb & 1u)) = w;
      }
    }
    asm volatile("s_waitcnt lgkmcnt(0)" ::: "memory");
    char* obase = (char*)O + ((size_t)row0 * HQ + h) * 256;
    #pragma unroll
    for (int half = 0; half < 2; ++half) {
      u32x4 v[8];
      #pragma unroll
      for (int j = 0; j < 8; ++j) { const unsigned r = 32u * half + 4u * j + fqb; v[j] = *(const u32x4*)(stg + 256u * r + 16u * (frb ^ (r & 15u))); }
      #pragma unroll
      for (int j = 0; j < 8; ++j) { const unsigned r = 32u * half + 4u * j + fqb; st16_wt(obase + (r * QRS + 16u * frb), v[j]); }
    }
  }
  STAMP(3);
}
}
namespace gu {
constexpr int NF = 7;
constexpr int GU_DS = 4;
constexpr unsigned BUFB = 65536u, OFF_G = 32768u, OFF_U = 49152u, HRS = DM * 2u, WRS = FF * 2u;
constexpr int LDSB = 2 * 65536;
struct QG {
  unsigned vh[2], vw[2][2];
  unsigned vhsrc[4], vwsrc[4];
  unsigned m0h, m0w, sofh, sofw;
  __amdgpu_buffer_rsrc_t srdH, srdG, srdU;
  bf16x4 wlo[2][2 * NF], wup[2][2 * NF]; bf16x8 hf[2][4];
};
#define GU_ACLOB W4_ACLOB, W4_ACLOB_HI
template <int SET, int M, bool ZERO> DI void mfma(QG& q, const bf16x8 (&wf)[2 * NF]) {
  constexpr int f = M >> 2, m = M & 3, ao = 4 * (2 * NF * m + f);
  if constexpr (ZERO) asm volatile("v_mfma_f32_16x16x32_bf16 a[%c2:%c3], %0, %1, 0" :: "v"(wf[f]), "v"(q.hf[SET][m]), "i"(ao), "i"(ao + 3) : GU_ACLOB);
  else                asm volatile("v_mfma_f32_16x16x32_bf16 a[%c2:%c3], %0, %1, a[%c2:%c3]" :: "v"(wf[f]), "v"(q.hf[SET][m]), "i"(ao), "i"(ao + 3));
}
template <int KS, int I> DI void rd(QG& q) {
  if constexpr (I < 4) asm volatile("ds_read_b128 %0, %1 offset:%2" : "=v"(q.hf[KS][I]) : "v"(q.vh[KS]), "i"(2048 * I) : "memory");
  else { constexpr int f = (I - 4) >> 1, n = f % NF, off = (f / NF) * 16384 + 8192 * KS + 512 * (n >> 1);
    if constexpr (((I - 4) & 1) == 0) asm volatile("ds_read_b64_tr_b16 %0, %1 offset:%2" : "=v"(q.wlo[KS][f]) : "v"(q.vw[n & 1][0]), "i"(off) : "memory");
    else                              asm volatile("ds_read_b64_tr_b16 %0, %1 offset:%2" : "=v"(q.wup[KS][f]) : "v"(q.vw[n & 1][1]), "i"(off) : "memory"); }
}
template <int G> DI void m0set(QG& q) { asm volatile("s_mov_b32 m0, %0" :: "s"(G == 0 ? q.m0h : G == 1 ? q.m0h + 4096u : G == 2 ? q.m0w : q.m0w + 16384u) : "m0", "memory"); }
template <int P> DI void dma(QG& q) {
  if constexpr (P < 8)       asm volatile("buffer_load_dwordx4 %0, %1, %2 offen offset:%3 lds" :: "v"(q.vhsrc[P & 3]), "s"(q.srdH), "s"(q.sofh + HRS * 32u * (P >> 2)), "i"(1024 * (P & 3)) : "memory");
  else if constexpr (P < 12) asm volatile("buffer_load_dwordx4 %0, %1, %2 offen offset:%3 lds" :: "v"(q.vwsrc[P & 3]), "s"(q.srdG), "s"(q.sofw), "i"(1024 * (P & 3)) : "memory");
  else                       asm volatile("buffer_load_dwordx4 %0, %1, %2 offen offset:%3 lds" :: "v"(q.vwsrc[P & 3]), "s"(q.srdU), "s"(q.sofw), "i"(1024 * (P & 3)) : "memory");
}
#define GU_V(a, S_) "+v"(q.a[S_][0]), "+v"(q.a[S_][1]), "+v"(q.a[S_][2]), "+v"(q.a[S_][3]), "+v"(q.a[S_][4]), "+v"(q.a[S_][5]), "+v"(q.a[S_][6]), \
                    "+v"(q.a[S_][7]), "+v"(q.a[S_][8]), "+v"(q.a[S_][9]), "+v"(q.a[S_][10]), "+v"(q.a[S_][11]), "+v"(q.a[S_][12]), "+v"(q.a[S_][13])
template <int SET> DI void wait_set(QG& q, bf16x8 (&wf)[2 * NF]) {
  asm volatile("s_waitcnt lgkmcnt(0)" : GU_V(wlo, SET), "+v"(q.hf[SET][0]), "+v"(q.hf[SET][1]), "+v"(q.hf[SET][2]), "+v"(q.hf[SET][3]) :: "memory");
  asm volatile("" : GU_V(wup, SET) :: "memory");
  #pragma unroll
  for (int f = 0; f < 2 * NF; ++f) wf[f] = __builtin_shufflevector(q.wlo[SET][f], q.wup[SET][f], 0, 1, 2, 3, 4, 5, 6, 7);
  asm volatile("s_nop 1" ::: "memory");
  GAP();
}
template <int KS, bool NOMM, bool ZERO, bool DMA, int M> DI void gap(QG& q, const bf16x8 (&wf)[2 * NF]) {
  if constexpr (!NOMM) mfma<KS ^ 1, M, ZERO>(q, wf);
  if constexpr (M < 32) rd<KS, M>(q);
  { constexpr int G = 8 * NF * KS + M;
    if constexpr (DMA && G >= 1 && (G - 1) % GU_DS == 0 && (G - 1) / GU_DS < 16) dma<(G - 1) / GU_DS>(q);
    if constexpr (DMA && G % (4 * GU_DS) == 0 && G / (4 * GU_DS) < 4) m0set<G / (4 * GU_DS)>(q); }
  GAP();
}
template <int KS, bool NOMM, bool ZERO, bool DMA, int... Ms> DI void kstep(QG& q, const bf16x8 (&wf)[2 * NF], iseq<Ms...>) { (gap<KS, NOMM, ZERO, DMA, Ms>(q, wf), ...); }
template <bool FIRST, bool DMA> DI void period(QG& q, bf16x8 (&wf)[2 * NF]) {
  kstep<0, FIRST, false, DMA>(q, wf, make_iseq<8 * NF>{}); wait_set<0>(q, wf);
  kstep<1, false, FIRST, DMA>(q, wf, make_iseq<8 * NF>{}); wait_set<1>(q, wf);
}
template <int P> DI void dma_m(QG& q) { if constexpr ((P & 3) == 0) { m0set<P / 4>(q); asm volatile("s_nop 0" ::: "memory"); } dma<P>(q); }
template <int... Ps> DI void dma_all(QG& q, iseq<Ps...>) { (dma_m<Ps>(q), ...); }
template <int... Ms> DI void mfma_last(QG& q, const bf16x8 (&wf)[2 * NF], iseq<Ms...>) { (mfma<1, Ms, false>(q, wf), ...); }
template <int R> DI f32x4 acc_read4() {
  f32x4 o;
  asm volatile("v_accvgpr_read_b32 %0, a[%c4]\n\tv_accvgpr_read_b32 %1, a[%c5]\n\tv_accvgpr_read_b32 %2, a[%c6]\n\tv_accvgpr_read_b32 %3, a[%c7]"
               : "=v"(o[0]), "=v"(o[1]), "=v"(o[2]), "=v"(o[3]) : "i"(R), "i"(R + 1), "i"(R + 2), "i"(R + 3));
  return o;
}
template <int I> DI void epi1(char* stg, unsigned fr, unsigned fq) {
  constexpr int m = I / NF, n = I % NF;
  const f32x4 gv = acc_read4<4 * (2 * NF * m + n)>(), uv = acc_read4<4 * (2 * NF * m + NF + n)>();
  float r[4];
  #pragma unroll
  for (int j = 0; j < 4; ++j) r[j] = silu_f(gv[j]) * uv[j];
  u32x2 w = {cvtpk(r[0], r[1]), cvtpk(r[2], r[3])};
  *(u32x2*)(stg + 256u * (16u * m + fr) + 16u * ((2u * n + (fq >> 1)) ^ fr) + 8u * (fq & 1u)) = w;
}
template <int... Is> DI void epi(char* stg, unsigned fr, unsigned fq, iseq<Is...>) { (epi1<Is>(stg, fr, fq), ...); }

__global__ __launch_bounds__(256, 1)
void gemm_gu(const bf16_t* __restrict__ A, const bf16_t* __restrict__ Wg, const bf16_t* __restrict__ Wu, bf16_t* __restrict__ act
  ) {
  extern __shared__ __attribute__((aligned(16))) char lds[];
  STAMP(0);
  const unsigned ldsb = (unsigned)(size_t)lds, lane = threadIdx.x & 63u, wid = threadIdx.x >> 6, fr = lane & 15u, fq = lane >> 4, uw = rfl(wid);
  const int unit = (blockIdx.x & 7) * 32 + (blockIdx.x >> 3), pm = unit & 7, pn = unit >> 3;
  const int row0 = pm * 256 + 64 * (int)uw, colb = pn * (16 * NF * 2);
  QG q; bf16x8 wf[2 * NF];
  q.srdH = __builtin_amdgcn_make_buffer_rsrc((void*)((const char*)A + (size_t)row0 * HRS), (short)0, (int)(64 * HRS), SRD_WORD3);
  q.srdG = __builtin_amdgcn_make_buffer_rsrc((void*)((const char*)Wg + colb - 1024), (short)0, (int)(DM * WRS) - colb + 1024, SRD_WORD3);
  q.srdU = __builtin_amdgcn_make_buffer_rsrc((void*)((const char*)Wu + colb - 1024), (short)0, (int)(DM * WRS) - colb + 1024, SRD_WORD3);
  #pragma unroll
  for (int p = 0; p < 4; ++p) q.vhsrc[p] = HRS * (8u * p + (lane >> 3)) + 16u * ((lane & 7u) ^ ((4u * (p & 1) + fq) & 7u)) - 1024u * p;
  #pragma unroll
  for (int w = 0; w < 4; ++w) q.vwsrc[w] = 1024u + WRS * (8u * (w >> 1) + ((lane >> 2) & 7u)) + 128u * (w & 1) + 64u * (lane >> 5) + 16u * ((lane & 3u) ^ ((2u * (w >> 1) + (fq & 1u)) & 3u)) - 1024u * w;
  #pragma unroll
  for (int w = 1; w < 4; w += 2) if ((lane >> 5) && (((lane & 3u) ^ ((2u * (w >> 1) + (fq & 1u)) & 3u)) >= 2u)) q.vwsrc[w] = 0x7ff00000u;
  unsigned vh0[2], vw0[2][2];
  #pragma unroll
  for (int ks = 0; ks < 2; ++ks) vh0[ks] = ldsb + 8192u * wid + 128u * fr + 16u * ((4u * ks + fq) ^ ((fr >> 1) & 7u));
  #pragma unroll
  for (int n = 0; n < 2; ++n)
    #pragma unroll
    for (int t = 0; t < 2; ++t) vw0[n][t] = ldsb + OFF_G + 2048u * fq + 64u * (4u * t + (fr >> 2)) + 16u * ((2u * n + ((lane & 3u) >> 1)) ^ ((2u * fq + t) & 3u)) + 8u * (lane & 1u);
  auto stage_to = [&](unsigned buf, int tile) {
    q.m0h = ldsb + buf + 8192u * uw; q.m0w = ldsb + buf + OFF_G + 4096u * uw;
    q.sofh = 128u * (unsigned)tile; q.sofw = WRS * (64u * (unsigned)tile + 16u * uw); };
  auto read_from = [&](unsigned buf) {
    q.vh[0] = vh0[0] + buf; q.vh[1] = vh0[1] + buf;
    #pragma unroll
    for (int n = 0; n < 2; ++n) { q.vw[n][0] = vw0[n][0] + buf; q.vw[n][1] = vw0[n][1] + buf; } };
  stage_to(0u, 0); asm volatile("s_nop 4" ::: "memory"); dma_all(q, make_iseq<16>{});
  STAMP(1);
  read_from(0u); stage_to(BUFB, 1);
  asm volatile("s_waitcnt vmcnt(0)\n\ts_barrier" ::: "memory"); GAP();
  period<true, true>(q, wf);
  for (int p = 1; p < DM / 64 - 1; ++p) {
    const unsigned brd = (unsigned)(p & 1) * BUFB;
    read_from(brd); stage_to(brd ^ BUFB, p + 1);
    asm volatile("s_waitcnt vmcnt(0)\n\ts_barrier" ::: "memory"); GAP();
    period<false, true>(q, wf);
  }
  read_from(BUFB);
  asm volatile("s_waitcnt vmcnt(0)\n\ts_barrier" ::: "memory"); GAP();
  period<false, false>(q, wf);
  mfma_last(q, wf, make_iseq<8 * NF>{});
  asm volatile("s_nop 7\n\ts_nop 7" ::: "memory");
  STAMP(2);
  {
    unsigned lb;
    asm volatile("v_mbcnt_lo_u32_b32 %0, -1, 0\n\tv_mbcnt_hi_u32_b32 %0, -1, %0" : "=v"(lb));
    const unsigned frb = lb & 15u, fqb = lb >> 4;
    char* stg = lds + 16384u * wid;
    char* obase = (char*)act + (size_t)row0 * WRS + colb;
    auto flush = [&](unsigned m) {
      asm volatile("s_waitcnt lgkmcnt(0)" ::: "memory");
      if (frb < 2u * NF) {
        u32x4 v[4];
        #pragma unroll
        for (int j = 0; j < 4; ++j) { const unsigned r = 16u * m + 4u * j + fqb; v[j] = *(const u32x4*)(stg + 256u * r + 16u * (frb ^ (r & 15u))); }
        #pragma unroll
        for (int j = 0; j < 4; ++j) { const unsigned r = 16u * m + 4u * j + fqb; st16_wt(obase + (r * WRS + 16u * frb), v[j]); }
      } };
    epi(stg, frb, fqb, iseq<0, 1, 2, 3, 4, 5, 6>{});        flush(0);
    epi(stg, frb, fqb, iseq<7, 8, 9, 10, 11, 12, 13>{});    flush(1);
    epi(stg, frb, fqb, iseq<14, 15, 16, 17, 18, 19, 20>{}); flush(2);
    epi(stg, frb, fqb, iseq<21, 22, 23, 24, 25, 26, 27>{}); flush(3);
  }
  STAMP(3);
}
}
namespace s8 {
constexpr int SK_LDS = 3 * 49152;
template <unsigned HRS_> struct Q8 {
  static constexpr unsigned HRS = HRS_;
  unsigned vh[2], vw[2][2], vhsrc[4], vwsrc[2], m0h, m0w, sofh, sofw;
  __amdgpu_buffer_rsrc_t srdH, srdW;
  bf16x4 wlo[2][4], wup[2][4]; bf16x8 hf[2][4];
};
#define S8_ACLOB "a0","a1","a2","a3","a4","a5","a6","a7","a8","a9","a10","a11","a12","a13","a14","a15","a16","a17","a18","a19","a20","a21","a22","a23","a24","a25","a26","a27","a28","a29","a30","a31", \
                 "a32","a33","a34","a35","a36","a37","a38","a39","a40","a41","a42","a43","a44","a45","a46","a47","a48","a49","a50","a51","a52","a53","a54","a55","a56","a57","a58","a59","a60","a61","a62","a63"
template <int SET, int M, bool ZERO, class Q> DI void mfma(Q& q, const bf16x8 (&wf)[4]) {
  constexpr int n = M >> 2, m = M & 3, ao = 4 * (4 * m + n);
  if constexpr (ZERO) asm volatile("v_mfma_f32_16x16x32_bf16 a[%c2:%c3], %0, %1, 0" :: "v"(wf[n]), "v"(q.hf[SET][m]), "i"(ao), "i"(ao + 3) : S8_ACLOB);
  else                asm volatile("v_mfma_f32_16x16x32_bf16 a[%c2:%c3], %0, %1, a[%c2:%c3]" :: "v"(wf[n]), "v"(q.hf[SET][m]), "i"(ao), "i"(ao + 3));
}
template <int KS, int I, class Q> DI void rd(Q& q) {
  if constexpr (I < 4) asm volatile("ds_read_b128 %0, %1 offset:%2" : "=v"(q.hf[KS][I]) : "v"(q.vh[KS]), "i"(2048 * I) : "memory");
  else { constexpr int n = (I - 4) >> 1;
    if constexpr (((I - 4) & 1) == 0) asm volatile("ds_read_b64_tr_b16 %0, %1 offset:%2" : "=v"(q.wlo[KS][n]) : "v"(q.vw[n & 1][0]), "i"(8192 * KS + 512 * (n >> 1)) : "memory");
    else                              asm volatile("ds_read_b64_tr_b16 %0, %1 offset:%2" : "=v"(q.wup[KS][n]) : "v"(q.vw[n & 1][1]), "i"(8192 * KS + 512 * (n >> 1)) : "memory"); }
}
template <int G, class Q> DI void m0set(Q& q) { asm volatile("s_mov_b32 m0, %0" :: "s"(G == 0 ? q.m0h : q.m0w) : "m0", "memory"); }
template <int P, class Q> DI void dma(Q& q) {
  if constexpr (P < 4) asm volatile("buffer_load_dwordx4 %0, %1, %2 offen offset:%3 lds" :: "v"(q.vhsrc[P]), "s"(q.srdH), "s"(q.sofh), "i"(1024 * P) : "memory");
  else                 asm volatile("buffer_load_dwordx4 %0, %1, %2 offen offset:%3 lds" :: "v"(q.vwsrc[P - 4]), "s"(q.srdW), "s"(q.sofw), "i"(1024 * (P - 4)) : "memory");
}
template <int SET, class Q> DI void wait_set(Q& q, bf16x8 (&wf)[4]) {
  asm volatile("s_waitcnt lgkmcnt(0)" : "+v"(q.wlo[SET][0]), "+v"(q.wlo[SET][1]), "+v"(q.wlo[SET][2]), "+v"(q.wlo[SET][3]), "+v"(q.wup[SET][0]), "+v"(q.wup[SET][1]), "+v"(q.wup[SET][2]), "+v"(q.wup[SET][3]),
                                        "+v"(q.hf[SET][0]), "+v"(q.hf[SET][1]), "+v"(q.hf[SET][2]), "+v"(q.hf[SET][3]) :: "memory");
  #pragma unroll
  for (int n = 0; n < 4; ++n) wf[n] = __builtin_shufflevector(q.wlo[SET][n], q.wup[SET][n], 0, 1, 2, 3, 4, 5, 6, 7);
  asm volatile("s_nop 1" ::: "memory");
  GAP();
}
template <int KS, bool NOMM, bool ZERO, bool DMA, int M, class Q> DI void gap(Q& q, const bf16x8 (&wf)[4]) {
  if constexpr (!NOMM) mfma<KS ^ 1, M, ZERO>(q, wf);
  if constexpr (M < 6) { rd<KS, 2 * M>(q); rd<KS, 2 * M + 1>(q); }
  if constexpr (DMA) {
    if constexpr (KS == 0) { if constexpr (M == 6) m0set<0>(q); if constexpr (M == 8 || M == 11 || M == 14) dma<(M - 8) / 3>(q); }
    else { if constexpr (M == 7) dma<3>(q); if constexpr (M == 9) m0set<1>(q); if constexpr (M == 11 || M == 14) dma<4 + (M - 11) / 3>(q); }
  }
  GAP();
}
template <int KS, bool NOMM, bool ZERO, bool DMA, class Q, int... Ms> DI void kstep(Q& q, const bf16x8 (&wf)[4], iseq<Ms...>) { (gap<KS, NOMM, ZERO, DMA, Ms>(q, wf), ...); }
template <bool FIRST, bool DMA, class Q> DI void period(Q& q, bf16x8 (&wf)[4]) {
  kstep<0, FIRST, false, DMA>(q, wf, make_iseq<16>{}); wait_set<0>(q, wf);
  kstep<1, false, FIRST, DMA>(q, wf, make_iseq<16>{}); wait_set<1>(q, wf);
}
template <int P, class Q> DI void dma_m(Q& q) { if constexpr (P == 0 || P == 4) { m0set<P / 4>(q); asm volatile("s_nop 0" ::: "memory"); } dma<P>(q); }
template <class Q, int... Ps> DI void dma_all(Q& q, iseq<Ps...>) { (dma_m<Ps>(q), ...); }
template <class Q, int... Ms> DI void mfma_last(Q& q, const bf16x8 (&wf)[4], iseq<Ms...>) { (mfma<1, Ms, false>(q, wf), ...); }

template <int NP, unsigned LDAB>
__global__ __launch_bounds__(512, 2)
void gemm_sk8(const bf16_t* __restrict__ A, const bf16_t* __restrict__ B, bf16_t* __restrict__ slab
  ) {
  extern __shared__ __attribute__((aligned(16))) char lds[];
  STAMP(0);
  constexpr unsigned WRS = DM * 2u, BUF = 49152u;
  const unsigned ldsb = (unsigned)(size_t)lds, lane = threadIdx.x & 63u, wid = threadIdx.x >> 6, fr = lane & 15u, fq = lane >> 4, uw = rfl(wid), wm = wid & 3u, wn = wid >> 2;
  const int xg = blockIdx.x & 7, in = blockIdx.x >> 3, split = xg >> 1, pm = 4 * (xg & 1) + (in & 3), pn = in >> 2;
  Q8<LDAB> q; bf16x8 wf[4];
  q.srdH = __builtin_amdgcn_make_buffer_rsrc((void*)((const char*)A + (size_t)(pm * 256 + 32 * (int)uw) * LDAB + (size_t)split * NP * 128), (short)0, (int)(32 * LDAB), SRD_WORD3);
  q.srdW = __builtin_amdgcn_make_buffer_rsrc((void*)((const char*)B + ((size_t)split * NP * 64 * DM + pn * 128) * 2 - 1024), (short)0, NP * 64 * DM * 2 - pn * 256 + 1024, SRD_WORD3);
  #pragma unroll
  for (int p = 0; p < 4; ++p) q.vhsrc[p] = LDAB * (8u * p + (lane >> 3)) + 16u * ((lane & 7u) ^ ((4u * (p & 1) + fq) & 7u)) - 1024u * p;
  #pragma unroll
  for (int w = 0; w < 2; ++w) q.vwsrc[w] = 1024u + WRS * ((lane >> 2) & 7u) + 128u * w + 64u * (lane >> 5) + 16u * ((lane & 3u) ^ ((2u * (wid & 1u) + (fq & 1u)) & 3u)) - 1024u * w;
  unsigned vh0[2], vw0[2][2];
  #pragma unroll
  for (int ks = 0; ks < 2; ++ks) vh0[ks] = ldsb + QBUF_H + 8192u * wm + 128u * fr + 16u * ((4u * ks + fq) ^ ((fr >> 1) & 7u));
  #pragma unroll
  for (int n = 0; n < 2; ++n)
    #pragma unroll
    for (int t = 0; t < 2; ++t) vw0[n][t] = ldsb + QBUF_W + 2048u * fq + 1024u * wn + 64u * (4u * t + (fr >> 2)) + 16u * ((2u * n + ((lane & 3u) >> 1)) ^ ((2u * fq + t) & 3u)) + 8u * (lane & 1u);
  auto stage_to = [&](unsigned buf, int tl) {
    q.m0h = ldsb + buf + QBUF_H + 4096u * uw; q.m0w = ldsb + buf + QBUF_W + 2048u * uw;
    q.sofh = 128u * (unsigned)tl; q.sofw = WRS * (64u * (unsigned)tl + 8u * uw); };
  auto read_from = [&](unsigned buf) {
    q.vh[0] = vh0[0] + buf; q.vh[1] = vh0[1] + buf;
    #pragma unroll
    for (int n = 0; n < 2; ++n) { q.vw[n][0] = vw0[n][0] + buf; q.vw[n][1] = vw0[n][1] + buf; } };
  STAMP(1);
  stage_to(0u, 0); asm volatile("s_nop 4" ::: "memory"); dma_all(q, make_iseq<6>{});
  stage_to(BUF, 1); dma_all(q, make_iseq<6>{});
  read_from(0u); stage_to(2 * BUF, 2);
  asm volatile("s_waitcnt vmcnt(6)\n\ts_barrier" ::: "memory"); GAP();
  period<true, true>(q, wf);
  unsigned brd = BUF, bst = 0u, bnx = 2 * BUF;
  for (int p = 1; p < NP - 2; ++p) {
    read_from(brd); stage_to(bst, p + 2);
    asm volatile("s_waitcnt vmcnt(6)\n\ts_barrier" ::: "memory"); GAP();
    period<false, true>(q, wf);
    const unsigned tmp = brd; brd = bnx; bnx = bst; bst = tmp;
  }
  read_from(brd);
  asm volatile("s_waitcnt vmcnt(6)\n\ts_barrier" ::: "memory"); GAP();
  period<false, false>(q, wf);
  read_from(bnx);
  asm volatile("s_waitcnt vmcnt(0)\n\ts_barrier" ::: "memory"); GAP();
  period<false, false>(q, wf);
  mfma_last(q, wf, make_iseq<16>{});
  asm volatile("s_nop 7\n\ts_nop 7" ::: "memory");
  STAMP(2);
  {
    unsigned lb;
    asm volatile("v_mbcnt_lo_u32_b32 %0, -1, 0\n\tv_mbcnt_hi_u32_b32 %0, -1, %0" : "=v"(lb));
    const unsigned frb = lb & 15u, fqb = lb >> 4;
    char* stg = lds + bst + 6144u * wid;
    char* obase = (char*)slab + (((size_t)split * S + pm * 256 + 64 * (wid & 3u)) * DM + pn * 128 + 64 * (wid >> 2)) * 2;
    #pragma unroll
    for (int half = 0; half < 2; ++half) {
      #pragma unroll
      for (int mm = 0; mm < 2; ++mm) {
        float o[16];
        W4_READ_O(half, mm, o);
        const unsigned r = 16u * mm + frb;
        #pragma unroll
        for (int n = 0; n < 4; ++n) {
          u32x2 w = {cvtpk(o[4 * n + 0], o[4 * n + 1]), cvtpk(o[4 * n + 2], o[4 * n + 3])};
          *(u32x2*)(stg + 128u * r + 16u * ((2u * n + (fqb >> 1)) ^ (r & 7u)) + 8u * (fqb & 1u)) = w;
        }
      }
      asm volatile("s_waitcnt lgkmcnt(0)" ::: "memory");
      u32x4 v[4];
      #pragma unroll
      for (int j = 0; j < 4; ++j) { const unsigned r = 8u * j + (lb >> 3); v[j] = *(const u32x4*)(stg + 128u * r + 16u * ((lb & 7u) ^ (r & 7u))); }
      #pragma unroll
      for (int j = 0; j < 4; ++j) { const unsigned r = 32u * half + 8u * j + (lb >> 3); st16_wt(obase + (r * (DM * 2u) + 16u * (lb & 7u)), v[j]); }
      asm volatile("s_waitcnt lgkmcnt(0)" ::: "memory");
    }
  }
  STAMP(3);
}
}
__device__ __forceinline__ float bfr(float x) { return (float)(__bf16)x; }
__device__ __forceinline__ float bf_lo(unsigned w) { return __uint_as_float(w << 16); }
__device__ __forceinline__ float bf_hi(unsigned w) { return __uint_as_float(w & 0xffff0000u); }
template <int CTRL> __device__ __forceinline__ float dpp_add(float v) { return v + __uint_as_float(__builtin_amdgcn_update_dpp(0u, __float_as_uint(v), CTRL, 0xf, 0xf, true)); }
__device__ __forceinline__ float wave_sum(float v) {
  v = dpp_add<0xB1>(v);
  v = dpp_add<0x4E>(v);
  v = dpp_add<0x141>(v);
  v = dpp_add<0x140>(v);
  { auto r = __builtin_amdgcn_permlane16_swap(__float_as_uint(v), __float_as_uint(v), false, false); v = __uint_as_float(r[0]) + __uint_as_float(r[1]); }
  { auto r = __builtin_amdgcn_permlane32_swap(__float_as_uint(v), __float_as_uint(v), false, false); v = __uint_as_float(r[0]) + __uint_as_float(r[1]); }
  return v;
}
__global__ __launch_bounds__(1024) void k_mod(const bf16_t* __restrict__ cvec, const bf16_t* __restrict__ wmod, float* __restrict__ mod) {
  __shared__ __attribute__((aligned(16))) float red[16][256];
  if (blockIdx.x >= 6 * DM / 32) {
    const unsigned w = __builtin_amdgcn_readfirstlane(threadIdx.x >> 6), l16 = (threadIdx.x & 63) * 16;
#define CODE_PF(sym, kib) do { const char* p_; asm volatile("s_getpc_b64 vcc\n\ts_add_u32 vcc_lo, vcc_lo, " sym "@rel32@lo+4\n\ts_addc_u32 vcc_hi, vcc_hi, " sym "@rel32@hi+12\n\ts_mov_b64 %0, vcc" : "=s"(p_) :: "scc", "vcc"); \
      for (unsigned i = w; i < (kib); i += 16) __builtin_amdgcn_global_load_lds((const unsigned*)(p_ + i * 1024 + l16), (__attribute__((address_space(3))) unsigned*)&red[w][0], 16, 0, 0); } while (0)
    CODE_PF("_Z5k_ln1PKtPKfPt", 2);
    CODE_PF("_ZN3a168attn_fwdEPKtS1_S1_S1_PtPKcS4_S4_S4_", 44);
    CODE_PF("_ZN2s88gemm_sk8ILi16ELj8192EEEvPKtS2_Pt", 4);
    CODE_PF("_Z9k_red_ln2ILi4EEvPKtS1_PKfPtS4_", 3);
    CODE_PF("_ZN2gu7gemm_guEPKtS1_S1_Pt", 12);
    CODE_PF("_ZN2s88gemm_sk8ILi14ELj7168EEEvPKtS2_Pt", 4);
    return;
  }
  const int tid = threadIdx.x, ch = tid & 3, r = tid >> 2, col0 = blockIdx.x * 32 + ch * 8;
  u32x4 w[4]; float s[4];
#pragma unroll
  for (int p = 0; p < 4; ++p) w[p] = *(const u32x4*)(wmod + (size_t)(r + 256 * p) * (6 * DM) + col0);
#pragma unroll
  for (int p = 0; p < 4; ++p) { const float cv = __uint_as_float((unsigned)cvec[r + 256 * p] << 16); s[p] = bfr(cv / (1.0f + __expf(-cv))); }
  float a[8];
#pragma unroll
  for (int j = 0; j < 8; ++j) a[j] = 0.f;
#pragma unroll
  for (int p = 0; p < 4; ++p)
#pragma unroll
    for (int e = 0; e < 4; ++e) { a[2 * e] += s[p] * bf_lo(w[p][e]); a[2 * e + 1] += s[p] * bf_hi(w[p][e]); }
#pragma unroll
  for (int j = 0; j < 8; ++j) {
    float v = dpp_add<0x124>(a[j]);
    v = dpp_add<0x128>(v);
    { auto r = __builtin_amdgcn_permlane16_swap(__float_as_uint(v), __float_as_uint(v), false, false); v = __uint_as_float(r[0]) + __uint_as_float(r[1]); }
    { auto r = __builtin_amdgcn_permlane32_swap(__float_as_uint(v), __float_as_uint(v), false, false); v = __uint_as_float(r[0]) + __uint_as_float(r[1]); }
    a[j] = v;
  }
  if ((tid & 63) < 4) {
#pragma unroll
    for (int j = 0; j < 8; ++j) red[tid >> 6][ch * 8 + j] = a[j];
  }
  __syncthreads();
  if (tid < 32) { float t = 0.f;
#pragma unroll
    for (int i = 0; i < 16; ++i) t += red[i][tid];
    mod[blockIdx.x * 32 + tid] = bfr(t); }
}
struct ModVec { f32x4 sc[4], sh[4]; };
__device__ __forceinline__ void load_modvec(ModVec& m, const float* __restrict__ shift, const float* __restrict__ scale, int lane) {
#pragma unroll
  for (int i = 0; i < 4; ++i) { const int c0 = (i >> 1) * 512 + lane * 8 + (i & 1) * 4; m.sc[i] = *(const f32x4*)(scale + c0); m.sh[i] = *(const f32x4*)(shift + c0); }
}
__device__ __forceinline__ void ln_mod_store(const float (&x)[16], const ModVec& m, bf16_t* __restrict__ hrow, int lane) {
  float s = 0.f;
#pragma unroll
  for (int j = 0; j < 16; ++j) s += x[j];
  const float mean = wave_sum(s) * (1.0f / DM);
  float q = 0.f;
#pragma unroll
  for (int j = 0; j < 16; ++j) { const float d = x[j] - mean; q += d * d; }
  const float var = bfr(wave_sum(q) * (1.0f / DM)), mb = bfr(mean);
  const float rsd = 1.0f / bfr(sqrtf(bfr(var + 1e-5f)));
#pragma unroll
  for (int hh = 0; hh < 2; ++hh) {
    float o[8];
#pragma unroll
    for (int j = 0; j < 8; ++j) { const float sc = m.sc[2 * hh + (j >> 2)][j & 3], sh = m.sh[2 * hh + (j >> 2)][j & 3];
      const float nrm = bfr(bfr(x[hh * 8 + j] - mb) * rsd); o[j] = bfr(nrm * bfr(1.0f + sc)) + sh; }
    u32x4 w = {cvtpk(o[0], o[1]), cvtpk(o[2], o[3]), cvtpk(o[4], o[5]), cvtpk(o[6], o[7])};
    st16_wt(hrow + hh * 512 + lane * 8, w);
  }
}
__global__ __launch_bounds__(256) void k_ln1(const bf16_t* __restrict__ x, const float* __restrict__ mod, bf16_t* __restrict__ h) {
  const int lane = threadIdx.x & 63, row = blockIdx.x * 4 + (threadIdx.x >> 6);
  u32x4 w[2]; ModVec mv;
#pragma unroll
  for (int hh = 0; hh < 2; ++hh) w[hh] = *(const u32x4*)(x + (size_t)row * DM + hh * 512 + lane * 8);
  load_modvec(mv, mod, mod + DM, lane);
  __builtin_amdgcn_sched_barrier(0);
  float v[16];
#pragma unroll
  for (int hh = 0; hh < 2; ++hh)
#pragma unroll
    for (int e = 0; e < 4; ++e) { v[hh * 8 + 2 * e] = bf_lo(w[hh][e]); v[hh * 8 + 2 * e + 1] = bf_hi(w[hh][e]); }
  ln_mod_store(v, mv, h + (size_t)row * DM, lane);
}
template <int NS> __global__ __launch_bounds__(256) void k_red_ln2(const bf16_t* __restrict__ slab, const bf16_t* __restrict__ x, const float* __restrict__ mod, bf16_t* __restrict__ x1, bf16_t* __restrict__ h) {
  const int lane = threadIdx.x & 63, row = blockIdx.x * 4 + (threadIdx.x >> 6);
  u32x4 p[NS][2], w[2]; f32x4 g[4]; ModVec mv;
#pragma unroll
  for (int s = 0; s < NS; ++s)
#pragma unroll
    for (int hh = 0; hh < 2; ++hh) p[s][hh] = *(const u32x4*)(slab + ((size_t)s * S + row) * DM + hh * 512 + lane * 8);
#pragma unroll
  for (int hh = 0; hh < 2; ++hh) w[hh] = *(const u32x4*)(x + (size_t)row * DM + hh * 512 + lane * 8);
#pragma unroll
  for (int i = 0; i < 4; ++i) g[i] = *(const f32x4*)(mod + 2 * DM + (i >> 1) * 512 + lane * 8 + (i & 1) * 4);
  load_modvec(mv, mod + 3 * DM, mod + 4 * DM, lane);
  __builtin_amdgcn_sched_barrier(0);
  float v[16];
#pragma unroll
  for (int hh = 0; hh < 2; ++hh) {
#pragma unroll
    for (int j = 0; j < 8; ++j) { float a = 0.f;
#pragma unroll
      for (int s = 0; s < NS; ++s) a += (j & 1) ? bf_hi(p[s][hh][j >> 1]) : bf_lo(p[s][hh][j >> 1]);
      const float xv = (j & 1) ? bf_hi(w[hh][j >> 1]) : bf_lo(w[hh][j >> 1]);
      v[hh * 8 + j] = bfr(xv + bfr(g[2 * hh + (j >> 2)][j & 3] * bfr(a))); }
    u32x4 o = {cvtpk(v[hh * 8 + 0], v[hh * 8 + 1]), cvtpk(v[hh * 8 + 2], v[hh * 8 + 3]), cvtpk(v[hh * 8 + 4], v[hh * 8 + 5]), cvtpk(v[hh * 8 + 6], v[hh * 8 + 7])};
    st16_wt(x1 + (size_t)row * DM + hh * 512 + lane * 8, o);
  }
  ln_mod_store(v, mv, h + (size_t)row * DM, lane);
}
template <int NS> __global__ __launch_bounds__(256) void k_red_out(const bf16_t* __restrict__ slab, const bf16_t* __restrict__ x1, const float* __restrict__ mod, bf16_t* __restrict__ out) {
  const size_t i = ((size_t)blockIdx.x * 256 + threadIdx.x) * 8; const int c0 = (int)(i & (DM - 1));
  u32x4 p[NS];
#pragma unroll
  for (int s = 0; s < NS; ++s) p[s] = *(const u32x4*)(slab + (size_t)s * S * DM + i);
  const u32x4 w = *(const u32x4*)(x1 + i);
  const f32x4 g0 = *(const f32x4*)(mod + 5 * DM + c0), g1 = *(const f32x4*)(mod + 5 * DM + c0 + 4);
  __builtin_amdgcn_sched_barrier(0);
  float v[8];
#pragma unroll
  for (int j = 0; j < 8; ++j) { float a = 0.f;
#pragma unroll
    for (int s = 0; s < NS; ++s) a += (j & 1) ? bf_hi(p[s][j >> 1]) : bf_lo(p[s][j >> 1]);
    const float xv = (j & 1) ? bf_hi(w[j >> 1]) : bf_lo(w[j >> 1]), gg = j < 4 ? g0[j & 3] : g1[j & 3];
    v[j] = xv + bfr(gg * bfr(a)); }
  u32x4 o = {cvtpk(v[0], v[1]), cvtpk(v[2], v[3]), cvtpk(v[4], v[5]), cvtpk(v[6], v[7])};
  st16_wt(out + i, o);
}
constexpr size_t WS_MOD = 0, WS_H = 65536, WS_O = WS_H + (size_t)S * DM * 2, WS_X1 = WS_O + (size_t)S * HQ * D * 2,
                 WS_ACT = WS_X1 + (size_t)S * DM * 2, WS_SLAB = WS_ACT + (size_t)S * FF * 2, WS_END = WS_SLAB + (size_t)4 * S * DM * 2;
#define STAMP_ARG(i)
extern "C" void kernel_launch(void* const* d_in, const int* in_sizes, int n_in, void* d_out, int out_size, void* d_ws, size_t ws_size, hipStream_t stream) {
  const bf16_t* x = (const bf16_t*)d_in[0]; const bf16_t* kc = (const bf16_t*)d_in[1]; const bf16_t* vc = (const bf16_t*)d_in[2];
  const bf16_t* wq = (const bf16_t*)d_in[3]; const bf16_t* wo = (const bf16_t*)d_in[4];
  const bf16_t* wg = (const bf16_t*)d_in[6]; const bf16_t* wu = (const bf16_t*)d_in[7]; const bf16_t* wd = (const bf16_t*)d_in[8];
  const bf16_t* cv = (const bf16_t*)d_in[9]; const bf16_t* wm = (const bf16_t*)d_in[10];
  char* ws = (char*)d_ws;
  float* mod = (float*)(ws + WS_MOD); bf16_t* h = (bf16_t*)(ws + WS_H); bf16_t* o = (bf16_t*)(ws + WS_O);
  bf16_t* x1 = (bf16_t*)(ws + WS_X1); bf16_t* act = (bf16_t*)(ws + WS_ACT); bf16_t* slab = (bf16_t*)(ws + WS_SLAB);
  (void)hipFuncSetAttribute((const void*)a16::attn_fwd, hipFuncAttributeMaxDynamicSharedMemorySize, (int)SHM_ATTN);
  (void)hipFuncSetAttribute((const void*)s8::gemm_sk8<16, HQ * D * 2u>, hipFuncAttributeMaxDynamicSharedMemorySize, s8::SK_LDS);
  (void)hipFuncSetAttribute((const void*)s8::gemm_sk8<14, FF * 2u>, hipFuncAttributeMaxDynamicSharedMemorySize, s8::SK_LDS);
  (void)hipFuncSetAttribute((const void*)gu::gemm_gu, hipFuncAttributeMaxDynamicSharedMemorySize, gu::LDSB);
  hipLaunchKernelGGL(k_mod, dim3(6 * DM / 32 + 8), dim3(1024), 0, stream, cv, wm, mod);
  hipLaunchKernelGGL(k_ln1, dim3(S / 4), dim3(256), 0, stream, x, (const float*)mod, h);
  hipLaunchKernelGGL(a16::attn_fwd, dim3(256), dim3(256), SHM_ATTN, stream, (const bf16_t*)h, wq, kc, vc, o, (const char*)wo, (const char*)wg, (const char*)wu, (const char*)wd STAMP_ARG(0));
  hipLaunchKernelGGL((s8::gemm_sk8<16, HQ * D * 2u>), dim3(256), dim3(512), s8::SK_LDS, stream, (const bf16_t*)o, wo, slab STAMP_ARG(2));
  hipLaunchKernelGGL(k_red_ln2<4>, dim3(S / 4), dim3(256), 0, stream, (const bf16_t*)slab, x, (const float*)mod, x1, h);
  hipLaunchKernelGGL(gu::gemm_gu, dim3(256), dim3(256), gu::LDSB, stream, (const bf16_t*)h, wg, wu, act STAMP_ARG(3));
  hipLaunchKernelGGL((s8::gemm_sk8<14, FF * 2u>), dim3(256), dim3(512), s8::SK_LDS, stream, (const bf16_t*)act, wd, slab STAMP_ARG(4));
  hipLaunchKernelGGL(k_red_out<4>, dim3(S * DM / 8 / 256), dim3(256), 0, stream, (const bf16_t*)slab, (const bf16_t*)x1, (const float*)mod, (bf16_t*)d_out);
}
```
